```python
import torch

def _softmax(x, axis=-1):
    return torch.softmax(x, dim=axis)
def _gelu(x):
    return torch.nn.functional.gelu(x, approximate="tanh")
def _silu(x):
    return torch.nn.functional.silu(x)
def _sigmoid(x):
    return torch.sigmoid(x)
def _layernorm(x, axis=-1, eps=1e-5):
    m = x.mean(dim=axis, keepdim=True)
    v = x.var(dim=axis, keepdim=True, unbiased=False)
    return (x - m) / torch.sqrt(v + eps)
def _scatter_add(tgt, idx, src, axis=0):
    return tgt.clone().index_add_(axis, idx, src)
def _segment_sum(src, seg, n):
    out = torch.zeros((n,) + src.shape[1:], dtype=src.dtype)
    return out.index_add_(0, seg, src)
def _pad(x, pw):
    flat = tuple(v for pair in reversed(pw) for v in pair)
    return torch.nn.functional.pad(x, flat)

kernel_name = 'standalone__detection_seg_fpn_lateral__vision'

def setup_inputs():

    _w = (lambda a: (lambda t: t.to(torch.bfloat16) if t.is_floating_point() else t)(torch.as_tensor(a)))
    H = W = (224 // 2) * 2
    C = 192
    k = 3
    return {
        "c_fine": _w((lambda *s: torch.randn(*s, dtype=torch.bfloat16) * 0.05)(1, H, W, C)),
        "p_coarse": _w((lambda *s: torch.randn(*s, dtype=torch.bfloat16) * 0.05)(1, H // 2, W // 2, C)),
        "w_lat": _w((lambda *s: torch.randn(*s, dtype=torch.bfloat16) * 0.05)(C, C)),
        "w_out": _w((lambda *s: torch.randn(*s, dtype=torch.bfloat16) * 0.05)(k, k, C, C)),
    }

def reference(c_fine, p_coarse, w_lat, w_out):
    B, H, W, C = c_fine.shape
    k = 3
    lat = torch.einsum("bhwc,cd->bhwd", c_fine, w_lat)
    up = (lambda x, n, axis: torch.repeat_interleave(x, n, dim=axis))((lambda x, n, axis: torch.repeat_interleave(x, n, dim=axis))(p_coarse, 2, axis=1), 2, axis=2)
    m = lat + up
    pad = k // 2
    mp = _pad(m, ((0, 0), (pad, pad), (pad, pad), (0, 0)))
    o = None
    for di in range(k):
        for dj in range(k):
            t = torch.einsum("bhwc,cd->bhwd", mp[:, di:di + H, dj:dj + W, :], w_out[di, dj])
            o = t if o is None else o + t
    return o
```

```cpp
#include <hip/hip_runtime.h>
#include <hip/hip_bf16.h>

typedef __attribute__((ext_vector_type(8))) short bf16x8;
typedef __attribute__((ext_vector_type(4))) float f32x4;

static __device__ __forceinline__ float bf2f(unsigned short v) { return __uint_as_float(((unsigned)v) << 16); }
static __device__ __forceinline__ unsigned short f2bf(float f) {
  __hip_bfloat16 b = __float2bfloat16(f);
  return *reinterpret_cast<unsigned short*>(&b);
}

constexpr int H = 224, W = 224, C = 192;

__global__ void lat_kernel(const unsigned short* __restrict__ cf, const unsigned short* __restrict__ pc,
                           const unsigned short* __restrict__ wl, unsigned short* __restrict__ m) {
  int p = blockIdx.x;
  int d = threadIdx.x;
  __shared__ float s[C];
  s[d] = bf2f(cf[(size_t)p * C + d]);
  __syncthreads();
  float acc = 0.f;
  for (int c = 0; c < C; ++c) acc += s[c] * bf2f(wl[c * C + d]);
  int y = p / W, x = p % W;
  float up = bf2f(pc[((size_t)(y / 2) * (W / 2) + x / 2) * C + d]);
  m[(size_t)p * C + d] = f2bf(acc + up);
}

__global__ void conv_kernel(const unsigned short* __restrict__ m, const unsigned short* __restrict__ wo,
                            unsigned short* __restrict__ out) {
  int p0 = blockIdx.x * 8;
  int d = threadIdx.x;
  __shared__ float s[3][10][C];
  int y = p0 / W, x0 = p0 % W;
  for (int r = 0; r < 3; ++r)
    for (int i = 0; i < 10; ++i) {
      int yy = y + r - 1, xx = x0 + i - 1;
      float v = 0.f;
      if (yy >= 0 && yy < H && xx >= 0 && xx < W) v = bf2f(m[((size_t)yy * W + xx) * C + d]);
      s[r][i][d] = v;
    }
  __syncthreads();
  float acc[8] = {0, 0, 0, 0, 0, 0, 0, 0};
  for (int di = 0; di < 3; ++di)
    for (int dj = 0; dj < 3; ++dj)
      for (int c = 0; c < C; ++c) {
        float w = bf2f(wo[((di * 3 + dj) * C + c) * C + d]);
#pragma unroll
        for (int i = 0; i < 8; ++i) acc[i] += s[di][i + dj][c] * w;
      }
  for (int i = 0; i < 8; ++i) out[(size_t)(p0 + i) * C + d] = f2bf(acc[i]);
}

extern "C" void kernel_launch(void* const* d_in, const int* in_sizes, int n_in, void* d_out, int out_size,
                              void* d_ws, size_t ws_size, hipStream_t stream) {
  const unsigned short* cf = (const unsigned short*)d_in[0];
  const unsigned short* pc = (const unsigned short*)d_in[1];
  const unsigned short* wl = (const unsigned short*)d_in[2];
  const unsigned short* wo = (const unsigned short*)d_in[3];
  unsigned short* m = (unsigned short*)d_ws;
  lat_kernel<<<H * W, C, 0, stream>>>(cf, pc, wl, m);
  conv_kernel<<<H * W / 8, C, 0, stream>>>(m, wo, (unsigned short*)d_out);
}
```

```cpp
#include <hip/hip_runtime.h>

namespace {
__global__ __launch_bounds__(256, 1) void fpn_main_kernel(const void* __restrict__ cf, const void* __restrict__ pc,
                                                          void* __restrict__ out, void* __restrict__ ws,
                                                          const void* __restrict__ wl, const void* __restrict__ wo) {
  asm volatile(
      "s_load_dwordx8 s[4:11], %1, 0x0\n\ts_load_dwordx4 s[12:15], %1, 0x20\n\t"
      "v_mov_b32_e32 v0, %0\n\t"
      "s_mov_b32 s77, %2\n\t"
      "v_lshlrev_b32_e32 v250, 6, v0\n\t"
      "v_lshrrev_b32_e32 v1, 6, v0\n\t"
      "s_lshr_b32 s80, s77, 3\n\t"
      "s_mul_i32 s64, s80, 0x500\n\t"
      "v_add_u32_e32 v251, s64, v250\n\t"
      "s_add_u32 s64, s64, 0x500\n\t"
      "s_getpc_b64 s[48:49]\n\tLpc_%=:\n\ts_and_b32 s49, s49, 0xffff\n\ts_min_u32 s50, s64, Lend_%=-Lpc_%=\n\ts_mov_b32 s51, 0x20000\n\t"
      "buffer_load_dword v253, v251, s[48:51], 0 offen\n\t"
      "v_readfirstlane_b32 s56, v1\n\t"
      "s_mul_i32 s59, s80, 0x900\n\t"
      "s_add_u32 s86, s59, 0x900\n\t"
      "v_add_u32_e32 v7, s59, v250\n\t"
      "v_lshlrev_b32_e32 v252, 7, v0\n\t"
      "v_and_b32_e32 v0, 63, v0\n\t"
      "v_lshrrev_b32_e32 v9, 3, v0\n\t"
      "v_mov_b32_e32 v39, 0x80000000\n\t"
      "s_and_b32 s59, s77, 6\n\t"
      "s_lshl_b32 s59, s59, 1\n\t"
      "s_lshr_b32 s60, s77, 6\n\t"
      "s_add_u32 s57, s59, s60\n\t"
      "s_and_b32 s59, s77, 1\n\t"
      "s_lshl_b32 s59, s59, 3\n\t"
      "s_and_b32 s60, s80, 7\n\t"
      "s_add_u32 s58, s59, s60\n\t"
      "s_and_b32 s59, s56, 1\n\t"
      "s_lshl_b32 s59, s59, 3\n\t"
      "v_add_u32_e32 v1, s59, v9\n\t"
      "v_lshrrev_b32_e32 v2, 1, v1\n\t"
      "v_xor_b32_e32 v2, v2, v0\n\t"
      "v_and_b32_e32 v2, 7, v2\n\t"
      "s_mul_i32 s60, s58, 14\n\t"
      "s_sub_u32 s60, s60, 1\n\t"
      "v_add_u32_e32 v1, s60, v1\n\t"
      "v_cmp_gt_u32_e32 vcc, 0xe0, v1\n\t"
      "v_mul_u32_u24_e32 v3, 0x180, v1\n\t"
      "v_lshl_add_u32 v3, v2, 4, v3\n\t"
      "v_cndmask_b32_e32 v8, v39, v3, vcc\n\t"
      "s_mul_i32 s61, s57, 14\n\t"
      "s_lshr_b32 s62, s56, 1\n\t"
      "s_add_u32 s79, s61, s62\n\t"
      "s_sub_u32 s79, s79, 1\n\t"
      "s_mul_i32 s75, s62, 0x1800\n\t"
      "s_lshl_b32 s59, s59, 7\n\t"
      "s_add_u32 s75, s75, s59\n\t"
      "s_lshl_b32 s59, s56, 3\n\t"
      "v_add_u32_e32 v1, s59, v9\n\t"
      "v_lshrrev_b32_e32 v2, 1, v1\n\t"
      "v_xor_b32_e32 v2, v2, v0\n\t"
      "v_and_b32_e32 v2, 7, v2\n\t"
      "s_mul_i32 s60, s58, 21\n\t"
      "s_sub_u32 s60, s60, 3\n\t"
      "v_add_u32_e32 v3, s60, v1\n\t"
      "v_cmp_gt_u32_e32 vcc, 0x150, v3\n\t"
      "v_lshlrev_b32_e32 v4, 7, v3\n\t"
      "v_lshl_add_u32 v4, v2, 4, v4\n\t"
      "v_cndmask_b32_e32 v4, v39, v4, vcc\n\t"
      "v_cmp_gt_u32_e32 vcc, 27, v1\n\t"
      "s_mul_i32 s70, s57, 7\n\t"
      "s_lshl_b32 s39, s56, 10\n\t"
      "v_cndmask_b32_e32 v140, v39, v4, vcc\n\t"
      "s_waitcnt lgkmcnt(0)\n\t"
      "s_mov_b32 s84, s12\n\t"
      "s_and_b32 s85, s13, 0xffff\n\t"
      "s_mov_b32 s87, 0x20000\n\t"
      "buffer_load_dword v254, v7, s[84:87], 0 offen\n\t"
      "s_mov_b32 s40, s4\n\t"
      "s_and_b32 s41, s5, 0xffff\n\t"
      "s_mov_b32 s43, 0x20000\n\t"
      "s_cmp_lt_u32 s79, 0xe0\n\ts_cselect_b32 s42, 0x1260000, 0\n\ts_max_i32 s81, s79, 0\n\ts_mul_i32 s67, s81, 0x15000\n\t"
      "s_add_u32 m0, s75, 0xc000\n\ts_nop 0\n\tbuffer_load_dwordx4 v8, s[40:43], s67 offen nt lds\n\t"
      "s_add_u32 m0, s75, 0xc780\n\ts_nop 0\n\tbuffer_load_dwordx4 v8, s[40:43], s67 offen offset:128 nt lds\n\t"
      "s_add_u32 m0, s75, 0xcf00\n\ts_nop 0\n\tbuffer_load_dwordx4 v8, s[40:43], s67 offen offset:256 nt lds\n\t"
      "s_mov_b32 s44, s6\n\t"
      "s_and_b32 s45, s7, 0xffff\n\t"
      "s_mov_b32 s47, 0x20000\n\t"
      "s_sub_u32 s82, s70, 1\n\ts_cmp_lt_u32 s82, 0x70\n\ts_cselect_b32 s46, 0x498000, 0\n\ts_max_i32 s82, s82, 0\n\ts_mul_i32 s68, s82, 0xa800\n\t"
      "s_add_u32 m0, s39, 0x23000\n\ts_nop 0\n\tbuffer_load_dwordx4 v140, s[44:47], s68 offen nt lds\n\t"
      "s_mov_b32 s46, 0x498000\n\ts_add_u32 s82, s70, 0\n\ts_mul_i32 s68, s82, 0xa800\n\t"
      "s_add_u32 m0, s39, 0x22000\n\ts_nop 0\n\tbuffer_load_dwordx4 v140, s[44:47], s68 offen nt lds\n\t"
      "s_mov_b32 s88, s14\n\t"
      "s_and_b32 s89, s15, 0xffff\n\t"
      "s_mov_b32 s90, 0xa2000\n\t"
      "s_mov_b32 s91, 0x20000\n\t"
      "v_and_b32_e32 v3, 3, v9\n\t"
      "v_mul_u32_u24_e32 v3, 0x180, v3\n\t"
      "v_and_b32_e32 v4, 1, v0\n\t"
      "v_lshl_add_u32 v3, v4, 2, v3\n\t"
      "v_lshrrev_b32_e32 v4, 5, v0\n\t"
      "v_bfe_u32 v5, v0, 1, 2\n\t"
      "s_lshl_b32 s59, s56, 6\n\ts_lshr_b32 s71, s77, 2\n\ts_add_u32 s59, s59, s71\n\ts_and_b32 s93, s77, 3\n\ts_cmp_lt_u32 s59, 0x90\n\ts_cselect_b32 s83, 0, 0x80000000\n\ts_cselect_b32 s59, s59, 0\n\ts_cmp_lt_u32 s59, 0x48\n\ts_cselect_b32 s37, 0, 0x48\n\ts_sub_u32 s59, s59, s37\n\ts_mul_i32 s94, s59, 43\n\ts_lshr_b32 s94, s94, 10\n\ts_mul_i32 s92, s94, 24\n\ts_sub_u32 s92, s59, s92\n\ts_lshr_b32 s71, s92, 2\n\ts_lshl_b32 s71, s71, 5\n\ts_lshl_b32 s65, s92, 2\n\ts_add_u32 s71, s71, s65\n\ts_lshr_b32 s65, s92, 1\n\ts_lshl_b32 s65, s65, 5\n\ts_lshl_b32 s59, s92, 3\n\ts_add_u32 s65, s65, s59\n\ts_sub_u32 s65, s65, 0x160\n\ts_cmp_lt_u32 s92, 16\n\ts_cselect_b32 s71, s71, s65\n\ts_cselect_b32 s65, 16, 4\n\ts_mul_i32 s65, s65, 0x180\n\t"
      "v_mad_u32_u24 v209, v4, s65, v3\n\t"
      "s_mul_i32 s71, s71, 0x180\n\ts_mul_i32 s59, s93, 0x60\n\ts_add_u32 s71, s71, s59\n\ts_lshl_b32 s59, s94, 3\n\ts_cmp_lt_u32 s94, 2\n\ts_cselect_b32 s59, s59, 64\n\ts_cselect_b32 s65, 16, 8\n\ts_add_u32 s71, s71, s59\n\t"
      "v_mad_u32_u24 v209, v5, s65, v209\n\t"
      "v_or_b32_e32 v209, s83, v209\n\t"
      "buffer_load_dword v200, v209, s[88:91], s71 offen nt\n\t"
      "s_add_u32 s71, s71, 0x12000\n\t"
      "buffer_load_dword v201, v209, s[88:91], s71 offen nt\n\t"
      "s_add_u32 s71, s71, 0x12000\n\t"
      "buffer_load_dword v202, v209, s[88:91], s71 offen nt\n\t"
      "s_add_u32 s71, s71, 0x12000\n\t"
      "buffer_load_dword v203, v209, s[88:91], s71 offen nt\n\t"
      "s_add_u32 s71, s71, 0x12000\n\t"
      "buffer_load_dword v204, v209, s[88:91], s71 offen nt\n\t"
      "s_add_u32 s71, s71, 0x12000\n\t"
      "buffer_load_dword v205, v209, s[88:91], s71 offen nt\n\t"
      "s_add_u32 s71, s71, 0x12000\n\t"
      "buffer_load_dword v206, v209, s[88:91], s71 offen nt\n\t"
      "s_add_u32 s71, s71, 0x12000\n\t"
      "buffer_load_dword v207, v209, s[88:91], s71 offen nt\n\t"
      "s_add_u32 s71, s71, 0x12000\n\t"
      "buffer_load_dword v208, v209, s[88:91], s71 offen nt\n\t"
      "v_and_b32_e32 v2, 6, v9\n\t"
      "v_xor_b32_e32 v2, v2, v0\n\t"
      "v_and_b32_e32 v2, 7, v2\n\t"
      "v_lshrrev_b32_e32 v4, 5, v0\n\t"
      "v_and_b32_e32 v1, 3, v9\n\t"
      "v_lshl_add_u32 v1, v4, 3, v1\n\t"
      "s_lshr_b32 s59, s56, 1\n\t"
      "s_lshl_b32 s59, s59, 4\n\t"
      "s_and_b32 s60, s56, 1\n\t"
      "s_lshl_b32 s60, s60, 2\n\t"
      "s_add_u32 s59, s59, s60\n\t"
      "v_add_u32_e32 v1, s59, v1\n\t"
      "v_mul_u32_u24_e32 v1, 0x180, v1\n\t"
      "v_lshl_add_u32 v136, v2, 4, v1\n\t"
      "s_mul_i32 s78, s56, 0xc00\n\t"
      "s_mov_b32 s86, 0x12000\n\t"
      "v_and_b32_e32 v37, 15, v0\n\t"
      "v_lshrrev_b32_e32 v38, 4, v0\n\t"
      "v_lshlrev_b32_e32 v12, 4, v0\n\t"
      "v_lshrrev_b32_e32 v1, 1, v37\n\t"
      "v_xor_b32_e32 v2, v1, v38\n\t"
      "v_lshlrev_b32_e32 v2, 4, v2\n\t"
      "v_lshl_add_u32 v2, v37, 7, v2\n\t"
      "v_add_u32_e32 v10, 0xc000, v2\n\t"
      "v_xor_b32_e32 v11, 64, v10\n\t"
      "v_lshlrev_b32_e32 v236, 2, v38\n\t"
      "v_sub_u32_e32 v236, v1, v236\n\t"
      "v_add_u32_e32 v236, 16, v236\n\t"
      "v_and_b32_e32 v237, 1, v37\n\t"
      "v_lshlrev_b32_e32 v237, 4, v237\n\t"
      "v_mov_b32_e32 v238, 0x3f80\n\t"
      "v_lshlrev_b32_e32 v237, v237, v238\n\t"
      "v_mov_b32_e32 v239, 0\n\t"
      "v_cmp_eq_u32_e32 vcc, 16, v236\n\t"
      "s_nop 1\n\t"
      "v_cndmask_b32_e32 v3, v239, v237, vcc\n\t"
      "v_cmp_eq_u32_e32 vcc, 17, v236\n\t"
      "v_accvgpr_write_b32 a88, v3\n\t"
      "s_nop 0\n\t"
      "v_cndmask_b32_e32 v4, v239, v237, vcc\n\t"
      "v_cmp_eq_u32_e32 vcc, 18, v236\n\t"
      "v_accvgpr_write_b32 a89, v4\n\t"
      "s_nop 0\n\t"
      "v_cndmask_b32_e32 v3, v239, v237, vcc\n\t"
      "v_cmp_eq_u32_e32 vcc, 19, v236\n\t"
      "v_accvgpr_write_b32 a90, v3\n\t"
      "s_nop 0\n\t"
      "v_cndmask_b32_e32 v4, v239, v237, vcc\n\t"
      "v_cmp_eq_u32_e32 vcc, 8, v236\n\t"
      "v_accvgpr_write_b32 a91, v4\n\t"
      "s_nop 0\n\t"
      "v_cndmask_b32_e32 v3, v239, v237, vcc\n\t"
      "v_cmp_eq_u32_e32 vcc, 9, v236\n\t"
      "v_accvgpr_write_b32 a92, v3\n\t"
      "s_nop 0\n\t"
      "v_cndmask_b32_e32 v4, v239, v237, vcc\n\t"
      "v_cmp_eq_u32_e32 vcc, 10, v236\n\t"
      "v_accvgpr_write_b32 a93, v4\n\t"
      "s_nop 0\n\t"
      "v_cndmask_b32_e32 v3, v239, v237, vcc\n\t"
      "v_cmp_eq_u32_e32 vcc, 11, v236\n\t"
      "v_accvgpr_write_b32 a94, v3\n\t"
      "s_nop 0\n\t"
      "v_cndmask_b32_e32 v4, v239, v237, vcc\n\t"
      "v_accvgpr_write_b32 a95, v4\n\t"
      "v_bfe_u32 v1, v0, 2, 2\n\t"
      "v_and_b32_e32 v2, 1, v38\n\t"
      "v_lshl_add_u32 v1, v2, 2, v1\n\t"
      "v_lshrrev_b32_e32 v2, 1, v38\n\t"
      "v_mul_u32_u24_e32 v2, 0x1800, v2\n\t"
      "v_lshl_add_u32 v2, v1, 7, v2\n\t"
      "v_and_b32_e32 v3, 1, v0\n\t"
      "v_lshl_add_u32 v2, v3, 3, v2\n\t"
      "v_bfe_u32 v3, v0, 1, 1\n\t"
      "v_and_b32_e32 v1, 6, v1\n\t"
      "v_xor_b32_e32 v3, v3, v1\n\t"
      "s_mul_i32 s61, s56, 3\n\t"
      "s_add_u32 s59, s61, 0\n\t"
      "s_and_b32 s60, s59, 3\n\t"
      "s_lshl_b32 s60, s60, 1\n\t"
      "s_lshr_b32 s59, s59, 2\n\t"
      "s_lshl_b32 s59, s59, 10\n\t"
      "v_xor_b32_e32 v4, s60, v3\n\t"
      "v_lshl_add_u32 v4, v4, 4, v2\n\t"
      "v_add_u32_e32 v137, s59, v4\n\t"
      "s_add_u32 s59, s61, 1\n\t"
      "s_and_b32 s60, s59, 3\n\t"
      "s_lshl_b32 s60, s60, 1\n\t"
      "s_lshr_b32 s59, s59, 2\n\t"
      "s_lshl_b32 s59, s59, 10\n\t"
      "v_xor_b32_e32 v4, s60, v3\n\t"
      "v_lshl_add_u32 v4, v4, 4, v2\n\t"
      "v_add_u32_e32 v138, s59, v4\n\t"
      "s_add_u32 s59, s61, 2\n\t"
      "s_and_b32 s60, s59, 3\n\t"
      "s_lshl_b32 s60, s60, 1\n\t"
      "s_lshr_b32 s59, s59, 2\n\t"
      "s_lshl_b32 s59, s59, 10\n\t"
      "v_xor_b32_e32 v4, s60, v3\n\t"
      "v_lshl_add_u32 v4, v4, 4, v2\n\t"
      "v_add_u32_e32 v139, s59, v4\n\t"
      "v_add_u32_e32 v1, 1, v37\n\t"
      "v_lshrrev_b32_e32 v1, 1, v1\n\t"
      "v_mul_u32_u24_e32 v1, 3, v1\n\t"
      "s_mul_i32 s59, s56, 6\n\t"
      "v_add3_u32 v2, v38, s59, 0\n\t"
      "v_lshrrev_b32_e32 v3, 3, v2\n\t"
      "v_add_u32_e32 v3, v3, v1\n\t"
      "v_lshrrev_b32_e32 v4, 1, v3\n\t"
      "v_xor_b32_e32 v4, v4, v2\n\t"
      "v_and_b32_e32 v4, 7, v4\n\t"
      "v_lshlrev_b32_e32 v3, 7, v3\n\t"
      "v_lshl_add_u32 v3, v4, 4, v3\n\t"
      "v_add_u32_e32 v141, 0x1b000, v3\n\t"
      "v_add3_u32 v2, v38, s59, 2\n\t"
      "v_lshrrev_b32_e32 v3, 3, v2\n\t"
      "v_add_u32_e32 v3, v3, v1\n\t"
      "v_lshrrev_b32_e32 v4, 1, v3\n\t"
      "v_xor_b32_e32 v4, v4, v2\n\t"
      "v_and_b32_e32 v4, 7, v4\n\t"
      "v_lshlrev_b32_e32 v3, 7, v3\n\t"
      "v_lshl_add_u32 v3, v4, 4, v3\n\t"
      "v_add_u32_e32 v142, 0x1b000, v3\n\t"
      "v_and_b32_e32 v1, 7, v37\n\t"
      "v_min_u32_e32 v1, 6, v1\n\t"
      "v_lshrrev_b32_e32 v2, 3, v37\n\t"
      "v_lshlrev_b32_e32 v6, 1, v38\n\t"
      "v_sub_u32_e32 v6, v6, v1\n\t"
      "v_add_u32_e32 v6, 8, v6\n\t"
      "v_cmp_eq_u32_e32 vcc, 1, v2\n\t"
      "v_mov_b32_e32 v236, 0x3f80\n\t"
      "v_mov_b32_e32 v237, 0x3f800000\n\t"
      "v_cndmask_b32_e32 v240, v236, v237, vcc\n\t"
      "v_mov_b32_e32 v236, 0xbf80\n\t"
      "v_mov_b32_e32 v237, 0xbf800000\n\t"
      "v_cndmask_b32_e32 v241, v236, v237, vcc\n\t"
      "v_mov_b32_e32 v236, 0x3f800000\n\t"
      "v_mov_b32_e32 v237, 0xbf800000\n\t"
      "v_cndmask_b32_e32 v242, v236, v237, vcc\n\t"
      "v_mov_b32_e32 v236, 0x3f80\n\t"
      "v_mov_b32_e32 v237, 0x3f80\n\t"
      "v_cndmask_b32_e32 v243, v236, v237, vcc\n\t"
      "v_cmp_eq_u32_e32 vcc, 8, v6\n\t"
      "s_nop 1\n\t"
      "v_cndmask_b32_e32 v244, v239, v240, vcc\n\t"
      "v_cndmask_b32_e32 v246, v239, v242, vcc\n\t"
      "v_cmp_eq_u32_e32 vcc, 9, v6\n\t"
      "s_nop 1\n\t"
      "v_cndmask_b32_e32 v244, v244, v241, vcc\n\t"
      "v_cndmask_b32_e32 v246, v246, v243, vcc\n\t"
      "v_cmp_eq_u32_e32 vcc, 7, v6\n\t"
      "s_nop 1\n\t"
      "v_cndmask_b32_e32 v245, v239, v240, vcc\n\t"
      "v_cndmask_b32_e32 v247, v239, v242, vcc\n\t"
      "v_cmp_eq_u32_e32 vcc, 8, v6\n\t"
      "s_nop 1\n\t"
      "v_cndmask_b32_e32 v245, v245, v241, vcc\n\t"
      "v_cndmask_b32_e32 v247, v247, v243, vcc\n\t"
      "v_and_b32_e32 v7, 7, v37\n\t"
      "v_cmp_eq_u32_e32 vcc, 7, v7\n\t"
      "v_mov_b32_e32 v236, 0x80008000\n\t"
      "v_mov_b32_e32 v237, 0x40000000\n\t"
      "v_cndmask_b32_e32 v238, v239, v236, vcc\n\t"
      "v_cndmask_b32_e32 v7, v239, v237, vcc\n\t"
      "v_xor_b32_e32 v244, v238, v244\n\t"
      "v_accvgpr_write_b32 a72, v244\n\t"
      "v_accvgpr_write_b32 a80, v244\n\t"
      "v_accvgpr_write_b32 a82, v244\n\t"
      "v_xor_b32_e32 v236, 0x80008000, v244\n\t"
      "v_accvgpr_write_b32 a74, v236\n\t"
      "v_xor_b32_e32 v245, v238, v245\n\t"
      "v_accvgpr_write_b32 a73, v245\n\t"
      "v_accvgpr_write_b32 a81, v245\n\t"
      "v_accvgpr_write_b32 a83, v245\n\t"
      "v_xor_b32_e32 v236, 0x80008000, v245\n\t"
      "v_accvgpr_write_b32 a75, v236\n\t"
      "v_xor_b32_e32 v246, v238, v246\n\t"
      "v_accvgpr_write_b32 a76, v246\n\t"
      "v_accvgpr_write_b32 a84, v246\n\t"
      "v_accvgpr_write_b32 a86, v246\n\t"
      "v_xor_b32_e32 v236, 0x80008000, v246\n\t"
      "v_accvgpr_write_b32 a78, v236\n\t"
      "v_xor_b32_e32 v247, v238, v247\n\t"
      "v_accvgpr_write_b32 a77, v247\n\t"
      "v_accvgpr_write_b32 a85, v247\n\t"
      "v_accvgpr_write_b32 a87, v247\n\t"
      "v_xor_b32_e32 v236, 0x80008000, v247\n\t"
      "v_accvgpr_write_b32 a79, v236\n\t"
      "s_lshl_b32 s59, s56, 2\n\t"
      "v_add_u32_e32 v3, s59, v38\n\t"
      "s_lshl_b32 s59, s56, 1\n\t"
      "s_add_u32 s59, s59, 16\n\t"
      "v_lshrrev_b32_e32 v4, 1, v38\n\t"
      "v_add_u32_e32 v4, s59, v4\n\t"
      "v_and_b32_e32 v5, 1, v38\n\t"
      "v_lshlrev_b32_e32 v5, 3, v5\n\t"
      "v_mul_u32_u24_e32 v6, 0x1800, v2\n\t"
      "v_lshl_add_u32 v6, v1, 4, v6\n\t"
      "v_add_u32_e32 v6, v6, v7\n\t"
      "v_lshl_add_u32 v13, v3, 8, v6\n\t"
      "v_lshl_add_u32 v237, v4, 8, v6\n\t"
      "v_add_u32_e32 v20, v237, v5\n\t"
      "v_sub_u32_e32 v237, v20, v13\n\t"
      "s_mov_b32 s60, 0xbf00\n\t"
      "v_add_u32_e32 v14, 0x70, v13\n\t"
      "v_add_u32_e32 v21, v14, v237\n\t"
      "v_add_u32_e32 v238, 14, v1\n\t"
      "v_lshrrev_b32_e32 v238, 4, v238\n\t"
      "v_mad_u32_u24 v238, v238, s60, v13\n\t"
      "v_add_u32_e32 v15, 0xe0, v238\n\t"
      "v_add_u32_e32 v22, v15, v237\n\t"
      "v_add_u32_e32 v16, 0xc050, v13\n\t"
      "v_add_u32_e32 v23, v16, v237\n\t"
      "v_add_u32_e32 v238, 28, v1\n\t"
      "v_lshrrev_b32_e32 v238, 4, v238\n\t"
      "v_mad_u32_u24 v238, v238, s60, v13\n\t"
      "v_add_u32_e32 v17, 0x1c0, v238\n\t"
      "v_add_u32_e32 v24, v17, v237\n\t"
      "v_add_u32_e32 v18, 0x18030, v13\n\t"
      "v_add_u32_e32 v25, v18, v237\n\t"
      "v_add_u32_e32 v238, 42, v1\n\t"
      "v_lshrrev_b32_e32 v238, 4, v238\n\t"
      "v_mad_u32_u24 v238, v238, s60, v13\n\t"
      "v_add_u32_e32 v19, 0x2a0, v238\n\t"
      "v_add_u32_e32 v26, v19, v237\n\t"
      "v_mul_u32_u24_e32 v6, 0x180, v2\n\t"
      "v_add_u32_e32 v6, 0x24000, v6\n\t"
      "v_lshrrev_b32_e32 v7, 30, v7\n\t"
      "v_mul_u32_u24_e32 v7, 0xc00, v7\n\t"
      "v_add_u32_e32 v6, v6, v7\n\t"
      "v_lshl_add_u32 v236, v3, 4, v6\n\t"
      "v_lshl_add_u32 v237, v4, 4, v6\n\t"
      "v_add_u32_e32 v237, v237, v5\n\t"
      "v_cmp_eq_u32_e32 vcc, 6, v1\n\t"
      "v_mov_b32_e32 v239, 0x180\n\t"
      "v_mov_b32_e32 v238, 0x1800\n\t"
      "v_cndmask_b32_e32 v19, v19, v236, vcc\n\t"
      "v_cndmask_b32_e32 v26, v26, v237, vcc\n\t"
      "v_cndmask_b32_e32 v238, v238, v239, vcc\n\t"
      "v_mad_u32_u24 v27, v238, 2, v19\n\t"
      "v_mad_u32_u24 v30, v238, 2, v26\n\t"
      "v_mad_u32_u24 v28, v238, 4, v19\n\t"
      "v_mad_u32_u24 v31, v238, 4, v26\n\t"
      "v_mad_u32_u24 v29, v238, 6, v19\n\t"
      "v_mad_u32_u24 v32, v238, 6, v26\n\t"
      "v_lshlrev_b32_e32 v1, 8, v38\n\t"
      "v_lshl_add_u32 v33, v37, 4, v1\n\t"
      "v_add_u32_e32 v34, 0xc000, v33\n\t"
      "v_add_u32_e32 v35, 0x18000, v33\n\t"
      "v_lshrrev_b32_e32 v1, 1, v37\n\t"
      "v_mov_b32_e32 v237, 0xaaaaaaaa\n\t"
      "v_cmp_eq_u32_e32 vcc, 0, v1\n\t"
      "v_mov_b32_e32 v2, 0xaa200088\n\t"
      "s_nop 0\n\t"
      "v_cndmask_b32_e32 v237, v237, v2, vcc\n\t"
      "v_cmp_eq_u32_e32 vcc, 1, v1\n\t"
      "v_mov_b32_e32 v2, 0xa2a45022\n\t"
      "s_nop 0\n\t"
      "v_cndmask_b32_e32 v237, v237, v2, vcc\n\t"
      "v_cmp_eq_u32_e32 vcc, 2, v1\n\t"
      "v_mov_b32_e32 v2, 0x888a4498\n\t"
      "s_nop 0\n\t"
      "v_cndmask_b32_e32 v237, v237, v2, vcc\n\t"
      "v_cmp_eq_u32_e32 vcc, 3, v1\n\t"
      "v_mov_b32_e32 v2, 0x298a1462\n\t"
      "s_nop 0\n\t"
      "v_cndmask_b32_e32 v237, v237, v2, vcc\n\t"
      "v_and_b32_e32 v1, 1, v37\n\t"
      "v_mul_u32_u24_e32 v236, 0xaaaa, v1\n\t"
      "v_add_u32_e32 v236, 0x4444, v236\n\t"
      "v_lshlrev_b32_e32 v2, 5, v38\n\t"
      "s_mov_b32 s52, s10\n\t"
      "s_and_b32 s53, s11, 0xffff\n\t"
      "s_mov_b32 s54, 0x130100\n\t"
      "s_mov_b32 s55, 0x20000\n\t"
      "s_add_u32 s98, s52, 1245184\n\ts_addc_u32 s99, s53, 0\n\ts_load_dwordx2 s[34:35], s[98:99], 0x0\n\t"
      "s_mul_i32 s36, s93, 0x48000\n\t"
      "s_lshr_b32 s65, s92, 2\n\t"
      "s_mul_i32 s65, s65, 3\n\t"
      "s_add_u32 s65, s65, s94\n\t"
      "s_lshl_b32 s65, s65, 10\n\t"
      "s_add_u32 s36, s36, s65\n\t"
      "s_and_b32 s65, s92, 3\n\t"
      "s_lshl_b32 s65, s65, 8\n\t"
      "s_add_u32 s36, s36, s65\n\t"
      "s_cmp_eq_u32 s37, 0\n\ts_mov_b32 s92, 0x3f000000\n\ts_cselect_b32 s92, 0x3e800000, s92\n\ts_mov_b32 s74, 0\n\ts_cselect_b32 s74, 0x3e800000, s74\n\ts_mov_b32 s93, 0\n\ts_cselect_b32 s93, 0x3e800000, s93\n\ts_mov_b32 s69, 0\n\ts_cselect_b32 s69, 0xbe800000, s69\n\ts_mov_b32 s94, 0\n\ts_cselect_b32 s94, 0x3e800000, s94\n\ts_mov_b32 s63, 0xbf000000\n\ts_cselect_b32 s63, 0x3e800000, s63\n\ts_mov_b32 s95, 0x40000000\n\ts_cselect_b32 s95, 0x3f800000, s95\n\ts_mov_b32 s97, 0xfcaed98b\n\ts_cselect_b32 s97, 0x37521640, s97\n\t"
      "s_or_b32 s96, s95, 0x80000000\n\t"
      "s_mov_b32 s64, 0\n\t"
      "s_waitcnt vmcnt(14)\n\t"
      "s_add_u32 m0, s78, 0\n\ts_nop 0\n\tbuffer_load_dwordx4 v136, s[84:87], s64 offen lds\n\t"
      "s_add_u32 m0, s78, 0x380\n\ts_nop 0\n\tbuffer_load_dwordx4 v136, s[84:87], s64 offen offset:128 lds\n\t"
      "s_add_u32 m0, s78, 0x700\n\ts_nop 0\n\tbuffer_load_dwordx4 v136, s[84:87], s64 offen offset:256 lds\n\t"
      "s_mov_b32 s64, 0x3000\n\t"
      "s_add_u32 m0, s78, 0x3000\n\ts_nop 0\n\tbuffer_load_dwordx4 v136, s[84:87], s64 offen lds\n\t"
      "s_add_u32 m0, s78, 0x3380\n\ts_nop 0\n\tbuffer_load_dwordx4 v136, s[84:87], s64 offen offset:128 lds\n\t"
      "s_add_u32 m0, s78, 0x3700\n\ts_nop 0\n\tbuffer_load_dwordx4 v136, s[84:87], s64 offen offset:256 lds\n\t"
      "s_mov_b32 s64, 0x6000\n\t"
      "s_add_u32 m0, s78, 0x6000\n\ts_nop 0\n\tbuffer_load_dwordx4 v136, s[84:87], s64 offen lds\n\t"
      "s_waitcnt vmcnt(7)\n\t"
      "v_lshlrev_b32_e32 v148, 16, v200\n\t"
      "v_lshlrev_b32_e32 v149, 16, v201\n\t"
      "v_lshlrev_b32_e32 v150, 16, v202\n\t"
      "v_lshlrev_b32_e32 v151, 16, v203\n\t"
      "v_lshlrev_b32_e32 v152, 16, v204\n\t"
      "v_lshlrev_b32_e32 v153, 16, v205\n\t"
      "v_lshlrev_b32_e32 v154, 16, v206\n\t"
      "v_lshlrev_b32_e32 v155, 16, v207\n\t"
      "v_lshlrev_b32_e32 v156, 16, v208\n\t"
      "v_mul_f32_e32 v158, s92, v148\n\t"
      "v_mul_f32_e32 v159, s92, v149\n\t"
      "s_add_u32 m0, s78, 0x6380\n\ts_nop 0\n\tbuffer_load_dwordx4 v136, s[84:87], s64 offen offset:128 lds\n\t"
      "v_mul_f32_e32 v160, s92, v150\n\t"
      "v_fmac_f32_e32 v158, s93, v151\n\t"
      "v_fmac_f32_e32 v159, s93, v152\n\t"
      "v_fmac_f32_e32 v160, s93, v153\n\t"
      "v_fmac_f32_e32 v158, s94, v154\n\t"
      "v_fmac_f32_e32 v159, s94, v155\n\t"
      "v_fmac_f32_e32 v160, s94, v156\n\t"
      "v_add_f32_e32 v161, v158, v160\n\t"
      "v_mul_f32_e32 v164, s95, v158\n\t"
      "v_mul_f32_e32 v167, s96, v160\n\t"
      "v_add_f32_e32 v165, v161, v159\n\t"
      "s_add_u32 m0, s78, 0x6700\n\ts_nop 0\n\tbuffer_load_dwordx4 v136, s[84:87], s64 offen offset:256 lds\n\t"
      "v_sub_f32_e32 v166, v161, v159\n\t"
      "v_mul_f32_e32 v158, s74, v148\n\t"
      "v_mul_f32_e32 v159, s74, v149\n\t"
      "v_mul_f32_e32 v160, s74, v150\n\t"
      "v_fmac_f32_e32 v158, s69, v151\n\t"
      "v_fmac_f32_e32 v159, s69, v152\n\t"
      "v_fmac_f32_e32 v160, s69, v153\n\t"
      "v_fmac_f32_e32 v158, s63, v154\n\t"
      "v_fmac_f32_e32 v159, s63, v155\n\t"
      "v_fmac_f32_e32 v160, s63, v156\n\t"
      "v_add_f32_e32 v161, v158, v160\n\t"
      "s_mov_b32 s64, 0x9000\n\t"
      "s_add_u32 m0, s78, 0x9000\n\ts_nop 0\n\tbuffer_load_dwordx4 v136, s[84:87], s64 offen lds\n\t"
      "v_mul_f32_e32 v172, s95, v158\n\t"
      "v_mul_f32_e32 v175, s96, v160\n\t"
      "v_add_f32_e32 v173, v161, v159\n\t"
      "v_sub_f32_e32 v174, v161, v159\n\t"
      "v_and_b32_e32 v148, 0xffff0000, v200\n\t"
      "v_and_b32_e32 v149, 0xffff0000, v201\n\t"
      "v_and_b32_e32 v150, 0xffff0000, v202\n\t"
      "v_and_b32_e32 v151, 0xffff0000, v203\n\t"
      "v_and_b32_e32 v152, 0xffff0000, v204\n\t"
      "v_and_b32_e32 v153, 0xffff0000, v205\n\t"
      "v_and_b32_e32 v154, 0xffff0000, v206\n\t"
      "s_add_u32 m0, s78, 0x9380\n\ts_nop 0\n\tbuffer_load_dwordx4 v136, s[84:87], s64 offen offset:128 lds\n\t"
      "v_and_b32_e32 v155, 0xffff0000, v207\n\t"
      "v_and_b32_e32 v156, 0xffff0000, v208\n\t"
      "v_mul_f32_e32 v158, s92, v148\n\t"
      "v_mul_f32_e32 v159, s92, v149\n\t"
      "v_mul_f32_e32 v160, s92, v150\n\t"
      "v_fmac_f32_e32 v158, s93, v151\n\t"
      "v_fmac_f32_e32 v159, s93, v152\n\t"
      "v_fmac_f32_e32 v160, s93, v153\n\t"
      "v_fmac_f32_e32 v158, s94, v154\n\t"
      "v_fmac_f32_e32 v159, s94, v155\n\t"
      "v_fmac_f32_e32 v160, s94, v156\n\t"
      "s_add_u32 m0, s78, 0x9700\n\ts_nop 0\n\tbuffer_load_dwordx4 v136, s[84:87], s64 offen offset:256 lds\n\t"
      "v_add_f32_e32 v161, v158, v160\n\t"
      "v_mul_f32_e32 v168, s95, v158\n\t"
      "v_mul_f32_e32 v171, s96, v160\n\t"
      "v_add_f32_e32 v169, v161, v159\n\t"
      "v_sub_f32_e32 v170, v161, v159\n\t"
      "v_mul_f32_e32 v158, s74, v148\n\t"
      "v_mul_f32_e32 v159, s74, v149\n\t"
      "v_mul_f32_e32 v160, s74, v150\n\t"
      "v_fmac_f32_e32 v158, s69, v151\n\t"
      "v_fmac_f32_e32 v159, s69, v152\n\t"
      "v_fmac_f32_e32 v160, s69, v153\n\t"
      "s_mov_b32 s64, 0xc000\n\t"
      "s_add_u32 m0, s78, 0x18000\n\ts_nop 0\n\tbuffer_load_dwordx4 v136, s[84:87], s64 offen lds\n\t"
      "v_fmac_f32_e32 v158, s63, v154\n\t"
      "v_fmac_f32_e32 v159, s63, v155\n\t"
      "v_fmac_f32_e32 v160, s63, v156\n\t"
      "v_add_f32_e32 v161, v158, v160\n\t"
      "v_mul_f32_e32 v176, s95, v158\n\t"
      "v_mul_f32_e32 v179, s96, v160\n\t"
      "v_add_f32_e32 v177, v161, v159\n\t"
      "v_sub_f32_e32 v178, v161, v159\n\t"
      "s_lshl_b32 s62, s56, 10\n\ts_add_u32 s62, s62, 0x24000\n\ts_cmp_eq_u32 s56, 3\n\ts_cselect_b32 s62, 0x27c00, s62\n\t"
      "v_and_b32_e32 v148, 7, v0\n\t"
      "v_lshlrev_b32_e32 v148, 5, v148\n\t"
      "s_add_u32 m0, s78, 0x18380\n\ts_nop 0\n\tbuffer_load_dwordx4 v136, s[84:87], s64 offen offset:128 lds\n\t"
      "v_lshl_add_u32 v192, v9, 1, v148\n\t"
      "v_add_u32_e32 v192, s62, v192\n\t"
      "v_add_u32_e32 v193, s62, v12\n\t"
      "s_waitcnt lgkmcnt(0)\n\t"
      "v_cvt_pk_bf16_f32 v180, v164, v168\n\t"
      "v_cvt_pk_bf16_f32 v181, v165, v169\n\t"
      "v_cvt_pk_bf16_f32 v182, v166, v170\n\t"
      "v_cvt_pk_bf16_f32 v183, v167, v171\n\t"
      "ds_write_b16 v192, v180\n\t"
      "ds_write_b16_d16_hi v192, v180 offset:16\n\t"
      "ds_write_b16 v192, v181 offset:256\n\t"
      "s_add_u32 m0, s78, 0x18700\n\ts_nop 0\n\tbuffer_load_dwordx4 v136, s[84:87], s64 offen offset:256 lds\n\t"
      "ds_write_b16_d16_hi v192, v181 offset:272\n\t"
      "ds_write_b16 v192, v182 offset:512\n\t"
      "ds_write_b16_d16_hi v192, v182 offset:528\n\t"
      "ds_write_b16 v192, v183 offset:768\n\t"
      "ds_write_b16_d16_hi v192, v183 offset:784\n\t"
      "ds_read_b128 v[184:187], v193\n\t"
      "v_cvt_pk_bf16_f32 v180, v172, v176\n\t"
      "v_cvt_pk_bf16_f32 v181, v173, v177\n\t"
      "v_cvt_pk_bf16_f32 v182, v174, v178\n\t"
      "v_cvt_pk_bf16_f32 v183, v175, v179\n\t"
      "ds_write_b16 v192, v180\n\t"
      "s_mov_b32 s64, 0xf000\n\t"
      "s_add_u32 m0, s78, 0x24c00\n\ts_nop 0\n\tbuffer_load_dwordx4 v136, s[84:87], s64 offen lds\n\t"
      "ds_write_b16_d16_hi v192, v180 offset:16\n\t"
      "ds_write_b16 v192, v181 offset:256\n\t"
      "ds_write_b16_d16_hi v192, v181 offset:272\n\t"
      "ds_write_b16 v192, v182 offset:512\n\t"
      "ds_write_b16_d16_hi v192, v182 offset:528\n\t"
      "ds_write_b16 v192, v183 offset:768\n\t"
      "ds_write_b16_d16_hi v192, v183 offset:784\n\t"
      "ds_read_b128 v[188:191], v193\n\t"
      "v_lshlrev_b32_e32 v148, 2, v38\n\t"
      "v_bfe_u32 v149, s97, v148, 4\n\t"
      "v_mul_u32_u24_e32 v149, 0x4800, v149\n\t"
      "s_add_u32 m0, s78, 0x24f80\n\ts_nop 0\n\tbuffer_load_dwordx4 v136, s[84:87], s64 offen offset:128 lds\n\t"
      "v_lshl_add_u32 v149, v37, 4, v149\n\t"
      "v_or_b32_e32 v194, s83, v149\n\t"
      "v_add_u32_e32 v148, 16, v148\n\t"
      "v_bfe_u32 v149, s97, v148, 4\n\t"
      "v_mul_u32_u24_e32 v149, 0x4800, v149\n\t"
      "v_lshl_add_u32 v149, v37, 4, v149\n\t"
      "v_or_b32_e32 v195, s83, v149\n\t"
      "s_add_u32 m0, s78, 0x25300\n\ts_nop 0\n\tbuffer_load_dwordx4 v136, s[84:87], s64 offen offset:256 lds\n\t"
      "s_barrier\n\t"
      "s_mov_b32 s42, 0x1260000\n\ts_add_u32 s81, s79, 2\n\ts_mul_i32 s67, s81, 0x15000\n\t"
      "s_add_u32 m0, s75, 0xf000\n\ts_nop 0\n\tbuffer_load_dwordx4 v8, s[40:43], s67 offen nt lds\n\t"
      "s_add_u32 m0, s75, 0xf780\n\ts_nop 0\n\tbuffer_load_dwordx4 v8, s[40:43], s67 offen offset:128 nt lds\n\t"
      "s_add_u32 m0, s75, 0xff00\n\ts_nop 0\n\tbuffer_load_dwordx4 v8, s[40:43], s67 offen offset:256 nt lds\n\t"
      "s_add_u32 s82, s70, 1\n\ts_mul_i32 s68, s82, 0xa800\n\t"
      "s_add_u32 m0, s39, 0x21000\n\ts_nop 0\n\tbuffer_load_dwordx4 v140, s[44:47], s68 offen nt lds\n\t"
      "s_add_u32 s81, s79, 4\n\ts_mul_i32 s67, s81, 0x15000\n\t"
      "s_add_u32 m0, s75, 0x12000\n\ts_nop 0\n\tbuffer_load_dwordx4 v8, s[40:43], s67 offen nt lds\n\t"
      "s_add_u32 m0, s75, 0x12780\n\ts_nop 0\n\tbuffer_load_dwordx4 v8, s[40:43], s67 offen offset:128 nt lds\n\t"
      "s_add_u32 m0, s75, 0x12f00\n\ts_nop 0\n\tbuffer_load_dwordx4 v8, s[40:43], s67 offen offset:256 nt lds\n\t"
      "s_add_u32 s82, s70, 2\n\ts_mul_i32 s68, s82, 0xa800\n\t"
      "s_add_u32 m0, s39, 0x20000\n\ts_nop 0\n\tbuffer_load_dwordx4 v140, s[44:47], s68 offen nt lds\n\t"
      "s_barrier\n\t"
      "s_waitcnt lgkmcnt(0)\n\t"
      "buffer_store_dwordx4 v[184:187], v194, s[52:55], s36 offen sc1\n\t"
      "buffer_store_dwordx4 v[188:191], v195, s[52:55], s36 offen sc1\n\t"
      "s_waitcnt vmcnt(10)\n\t"
      "s_barrier\n\t"
      "s_add_u32 s82, s70, 3\n\ts_mul_i32 s68, s82, 0xa800\n\t"
      "s_add_u32 m0, s39, 0x1f000\n\t"
      "s_add_u32 s81, s79, 6\n\ts_mul_i32 s67, s81, 0x15000\n\t"
      "ds_read_b128 a[100:103], v141 offset:32768\n\t"
      "ds_read_b128 a[116:119], v142 offset:32768\n\t"
      "ds_read_b64_tr_b16 a[0:1], v137\n\t"
      "ds_read_b64_tr_b16 a[2:3], v137 offset:3072\n\t"
      "ds_read_b128 v[40:43], v10\n\t"
      "ds_read_b64_tr_b16 a[4:5], v138\n\t"
      "ds_read_b64_tr_b16 a[6:7], v138 offset:3072\n\t"
      "ds_read_b64_tr_b16 a[8:9], v139\n\t"
      "ds_read_b64_tr_b16 a[10:11], v139 offset:3072\n\t"
      "ds_read_b128 v[44:47], v11\n\t"
      "ds_read_b64_tr_b16 a[12:13], v137 offset:12288\n\t"
      "ds_read_b64_tr_b16 a[14:15], v137 offset:15360\n\t"
      "ds_read_b64_tr_b16 a[16:17], v138 offset:12288\n\t"
      "ds_read_b64_tr_b16 a[18:19], v138 offset:15360\n\t"
      "ds_read_b64_tr_b16 a[20:21], v139 offset:12288\n\t"
      "ds_read_b64_tr_b16 a[22:23], v139 offset:15360\n\t"
      "s_waitcnt lgkmcnt(15)\n\t"
      "ds_read_b128 v[48:51], v10 offset:2048\n\t"
      "v_mfma_f32_16x16x32_bf16 v[112:115], a[100:103], a[88:91], 0\n\t"
      "ds_read_b64_tr_b16 a[24:25], v137 offset:24576\n\t"
      "ds_read_b64_tr_b16 a[26:27], v137 offset:27648\n\t"
      "v_mfma_f32_16x16x32_bf16 v[116:119], a[100:103], a[92:95], 0\n\t"
      "s_waitcnt lgkmcnt(15)\n\t"
      "ds_read_b64_tr_b16 a[28:29], v138 offset:24576\n\t"
      "v_mfma_f32_16x16x32_bf16 v[120:123], a[116:119], a[92:95], 0\n\t"
      "s_waitcnt lgkmcnt(15)\n\t"
      "ds_read_b64_tr_b16 a[30:31], v138 offset:27648\n\t"
      "v_mfma_f32_16x16x32_bf16 v[88:91], v[40:43], a[0:3], v[112:115]\n\t"
      "s_waitcnt lgkmcnt(14)\n\t"
      "ds_read_b64_tr_b16 a[32:33], v139 offset:24576\n\t"
      "v_mfma_f32_16x16x32_bf16 v[92:95], v[40:43], a[4:7], v[116:119]\n\t"
      "s_waitcnt lgkmcnt(13)\n\t"
      "ds_read_b64_tr_b16 a[34:35], v139 offset:27648\n\t"
      "v_mfma_f32_16x16x32_bf16 v[96:99], v[40:43], a[8:11], v[120:123]\n\t"
      "s_waitcnt lgkmcnt(11)\n\t"
      "ds_read_b128 a[104:107], v141 offset:28672\n\t"
      "v_mfma_f32_16x16x32_bf16 v[88:91], v[44:47], a[12:15], v[88:91]\n\t"
      "s_waitcnt lgkmcnt(10)\n\t"
      "ds_read_b128 a[120:123], v142 offset:28672\n\t"
      "v_mfma_f32_16x16x32_bf16 v[92:95], v[44:47], a[16:19], v[92:95]\n\t"
      "s_waitcnt lgkmcnt(9)\n\t"
      "ds_read_b128 v[52:55], v11 offset:2048\n\t"
      "v_mfma_f32_16x16x32_bf16 v[96:99], v[44:47], a[20:23], v[96:99]\n\t"
      "ds_read_b64_tr_b16 a[36:37], v137 offset:36864\n\t"
      "s_waitcnt lgkmcnt(8)\n\t"
      "ds_read_b64_tr_b16 a[38:39], v137 offset:39936\n\t"
      "v_mfma_f32_16x16x32_bf16 v[88:91], v[48:51], a[24:27], v[88:91]\n\t"
      "ds_read_b64_tr_b16 a[40:41], v138 offset:36864\n\t"
      "ds_read_b64_tr_b16 a[42:43], v138 offset:39936\n\t"
      "s_waitcnt lgkmcnt(9)\n\t"
      "ds_read_b64_tr_b16 a[44:45], v139 offset:36864\n\t"
      "v_mfma_f32_16x16x32_bf16 v[92:95], v[48:51], a[28:31], v[92:95]\n\t"
      "ds_read_b64_tr_b16 a[46:47], v139 offset:39936\n\t"
      "v_add_u32_e32 v137, 0x18000, v137\n\t"
      "s_waitcnt lgkmcnt(9)\n\t"
      "ds_read_b128 v[56:59], v10 offset:4096\n\t"
      "v_mfma_f32_16x16x32_bf16 v[96:99], v[48:51], a[32:35], v[96:99]\n\t"
      "s_waitcnt lgkmcnt(9)\n\t"
      "ds_read_b64_tr_b16 a[48:49], v137\n\t"
      "v_mfma_f32_16x16x32_bf16 v[124:127], a[104:107], a[88:91], 0\n\t"
      "ds_read_b64_tr_b16 a[50:51], v137 offset:3072\n\t"
      "v_add_u32_e32 v138, 0x18000, v138\n\t"
      "v_mfma_f32_16x16x32_bf16 v[128:131], a[104:107], a[92:95], 0\n\t"
      "s_waitcnt lgkmcnt(10)\n\t"
      "ds_read_b64_tr_b16 a[52:53], v138\n\t"
      "v_mfma_f32_16x16x32_bf16 v[132:135], a[120:123], a[92:95], 0\n\t"
      "ds_read_b64_tr_b16 a[54:55], v138 offset:3072\n\t"
      "s_waitcnt lgkmcnt(9)\n\t"
      "v_add_u32_e32 v139, 0x18000, v139\n\t"
      "ds_read_b64_tr_b16 a[56:57], v139\n\t"
      "v_mfma_f32_16x16x32_bf16 v[88:91], v[52:55], a[36:39], v[88:91]\n\t"
      "s_waitcnt lgkmcnt(8)\n\t"
      "ds_read_b64_tr_b16 a[58:59], v139 offset:3072\n\t"
      "v_mfma_f32_16x16x32_bf16 v[92:95], v[52:55], a[40:43], v[92:95]\n\t"
      "ds_read_b128 v[60:63], v11 offset:4096\n\t"
      "s_waitcnt lgkmcnt(8)\n\t"
      "ds_read_b64_tr_b16 a[60:61], v137 offset:52224\n\t"
      "v_mfma_f32_16x16x32_bf16 v[96:99], v[52:55], a[44:47], v[96:99]\n\t"
      "ds_read_b64_tr_b16 a[62:63], v137 offset:55296\n\t"
      "ds_read_b64_tr_b16 a[64:65], v138 offset:52224\n\t"
      "ds_read_b64_tr_b16 a[66:67], v138 offset:55296\n\t"
      "s_waitcnt lgkmcnt(9)\n\t"
      "ds_read_b64_tr_b16 a[68:69], v139 offset:52224\n\t"
      "v_mfma_f32_16x16x32_bf16 v[88:91], v[56:59], a[48:51], v[88:91]\n\t"
      "ds_read_b64_tr_b16 a[70:71], v139 offset:55296\n\t"
      "ds_read_b128 v[64:67], v10 offset:6144\n\t"
      "s_waitcnt lgkmcnt(10)\n\t"
      "ds_read_b128 v[68:71], v11 offset:6144\n\t"
      "v_mfma_f32_16x16x32_bf16 v[92:95], v[56:59], a[52:55], v[92:95]\n\t"
      "ds_read_b128 v[72:75], v10 offset:8192\n\t"
      "ds_read_b128 v[76:79], v11 offset:8192\n\t"
      "ds_read_b128 v[80:83], v10 offset:10240\n\t"
      "s_waitcnt lgkmcnt(12)\n\t"
      "ds_read_b128 v[84:87], v11 offset:10240\n\t"
      "v_mfma_f32_16x16x32_bf16 v[96:99], v[56:59], a[56:59], v[96:99]\n\t"
      "s_mul_i32 s59, s57, 0x126000\n\t"
      "s_mul_i32 s60, s58, 0x1500\n\t"
      "v_lshl_add_u32 v2, v1, 2, v2\n\t"
      "v_cmp_eq_u32_e32 vcc, 0, v252\n\t"
      "s_waitcnt lgkmcnt(10)\n\t"
      "v_mov_b32_e32 v4, 0\n\t"
      "s_add_u32 s71, s59, s60\n\t"
      "v_mfma_f32_16x16x32_bf16 v[88:91], v[60:63], a[60:63], v[88:91]\n\t"
      "s_waitcnt lgkmcnt(8)\n\t"
      "s_lshl_b32 s38, s77, 7\n\t"
      "s_add_u32 s34, s34, 1\n\t"
      "v_mfma_f32_16x16x32_bf16 v[92:95], v[60:63], a[64:67], v[92:95]\n\t"
      "v_add_u32_e32 v238, 0x24000, v2\n\t"
      "s_waitcnt lgkmcnt(6)\n\t"
      "s_mov_b32 s76, 0xc00\n\t"
      "s_add_u32 s72, s71, 0x180\n\t"
      "v_mfma_f32_16x16x32_bf16 v[96:99], v[60:63], a[68:71], v[96:99]\n\t"
      "v_cvt_pk_bf16_f32 v88, v88, v89\n\t"
      "v_cvt_pk_bf16_f32 v89, v90, v91\n\t"
      "v_accvgpr_write_b32 a96, v88\n\t"
      "v_accvgpr_write_b32 a97, v89\n\t"
      "v_cvt_pk_bf16_f32 v92, v92, v93\n\t"
      "v_cvt_pk_bf16_f32 v93, v94, v95\n\t"
      "v_accvgpr_write_b32 a112, v92\n\t"
      "v_accvgpr_write_b32 a113, v93\n\t"
      "v_cvt_pk_bf16_f32 v96, v96, v97\n\t"
      "v_cvt_pk_bf16_f32 v97, v98, v99\n\t"
      "s_waitcnt vmcnt(6) lgkmcnt(0)\n\t"
      "v_accvgpr_write_b32 a128, v96\n\t"
      "v_accvgpr_write_b32 a129, v97\n\t"
      "s_barrier\n\t"
      "v_mfma_f32_16x16x32_bf16 v[100:103], v[64:67], a[0:3], v[124:127]\n\t"
      "ds_read_b128 v[40:43], v10 offset:12288\n\t"
      "s_add_u32 s73, s71, 0x15000\n\t"
      "v_mfma_f32_16x16x32_bf16 v[104:107], v[64:67], a[4:7], v[128:131]\n\t"
      "s_add_u32 s74, s71, 0x15180\n\t"
      "s_mul_i32 s69, s56, 0x48000\n\t"
      "s_mov_b32 s48, s8\n\t"
      "v_mfma_f32_16x16x32_bf16 v[108:111], v[64:67], a[8:11], v[132:135]\n\t"
      "ds_read_b128 v[44:47], v11 offset:12288\n\t"
      "s_and_b32 s49, s9, 0xffff\n\t"
      "v_mfma_f32_16x16x32_bf16 v[100:103], v[68:71], a[12:15], v[100:103]\n\t"
      "s_mov_b32 s50, 0x1260000\n\t"
      "s_mov_b32 s51, 0x20000\n\t"
      "v_cndmask_b32_e32 v253, v39, v4, vcc\n\t"
      "v_mfma_f32_16x16x32_bf16 v[104:107], v[68:71], a[16:19], v[104:107]\n\t"
      "ds_read_b128 v[48:51], v10 offset:14336\n\t"
      "s_add_u32 s38, s38, 0x120000\n\t"
      "v_mfma_f32_16x16x32_bf16 v[108:111], v[68:71], a[20:23], v[108:111]\n\t"
      "v_mov_b32_e32 v248, s34\n\t"
      "v_mov_b32_e32 v249, s35\n\t"
      "v_mfma_f32_16x16x32_bf16 v[100:103], v[72:75], a[24:27], v[100:103]\n\t"
      "ds_read_b128 a[136:139], v141 offset:24576\n\t"
      "s_mov_b32 s66, 0x120000\n\t"
      "v_mfma_f32_16x16x32_bf16 v[104:107], v[72:75], a[28:31], v[104:107]\n\t"
      "v_mfma_f32_16x16x32_bf16 v[108:111], v[72:75], a[32:35], v[108:111]\n\t"
      "ds_read_b128 a[152:155], v142 offset:24576\n\t"
      "v_mfma_f32_16x16x32_bf16 v[100:103], v[76:79], a[36:39], v[100:103]\n\t"
      "v_mfma_f32_16x16x32_bf16 v[104:107], v[76:79], a[40:43], v[104:107]\n\t"
      "buffer_load_dwordx4 v140, s[44:47], s68 offen nt lds\n\t"
      "v_mfma_f32_16x16x32_bf16 v[108:111], v[76:79], a[44:47], v[108:111]\n\t"
      "s_add_u32 s82, s70, 4\n\ts_mul_i32 s68, s82, 0xa800\n\t"
      "s_add_u32 m0, s39, 0x1e000\n\t"
      "v_mfma_f32_16x16x32_bf16 v[100:103], v[80:83], a[48:51], v[100:103]\n\t"
      "ds_read_b128 v[52:55], v11 offset:14336\n\t"
      "v_mfma_f32_16x16x32_bf16 v[104:107], v[80:83], a[52:55], v[104:107]\n\t"
      "v_mfma_f32_16x16x32_bf16 v[108:111], v[80:83], a[56:59], v[108:111]\n\t"
      "ds_read_b128 v[56:59], v10 offset:16384\n\t"
      "v_mfma_f32_16x16x32_bf16 v[100:103], v[84:87], a[60:63], v[100:103]\n\t"
      "v_mfma_f32_16x16x32_bf16 v[104:107], v[84:87], a[64:67], v[104:107]\n\t"
      "ds_read_b128 v[60:63], v11 offset:16384\n\t"
      "v_mfma_f32_16x16x32_bf16 v[108:111], v[84:87], a[68:71], v[108:111]\n\t"
      "s_waitcnt lgkmcnt(4)\n\t"
      "v_mfma_f32_16x16x32_bf16 v[88:91], v[40:43], a[0:3], v[124:127]\n\t"
      "buffer_load_dwordx4 v140, s[44:47], s68 offen nt lds\n\t"
      "v_mfma_f32_16x16x32_bf16 v[92:95], v[40:43], a[4:7], v[128:131]\n\t"
      "s_add_u32 m0, s75, 0x15000\n\t"
      "s_add_u32 s82, s70, 5\n\ts_mul_i32 s68, s82, 0xa800\n\t"
      "v_mfma_f32_16x16x32_bf16 v[96:99], v[40:43], a[8:11], v[132:135]\n\t"
      "ds_read_b128 v[64:67], v10 offset:18432\n\t"
      "v_cvt_pk_bf16_f32 v150, v100, v101\n\t"
      "v_mfma_f32_16x16x32_bf16 v[88:91], v[44:47], a[12:15], v[88:91]\n\t"
      "v_cvt_pk_bf16_f32 v151, v102, v103\n\t"
      "v_cvt_pk_bf16_f32 v154, v104, v105\n\t"
      "v_mfma_f32_16x16x32_bf16 v[92:95], v[44:47], a[16:19], v[92:95]\n\t"
      "ds_read_b128 v[68:71], v11 offset:18432\n\t"
      "v_cvt_pk_bf16_f32 v155, v106, v107\n\t"
      "v_mfma_f32_16x16x32_bf16 v[96:99], v[44:47], a[20:23], v[96:99]\n\t"
      "v_cvt_pk_bf16_f32 v158, v108, v109\n\t"
      "v_cvt_pk_bf16_f32 v159, v110, v111\n\t"
      "v_mfma_f32_16x16x32_bf16 v[88:91], v[48:51], a[24:27], v[88:91]\n\t"
      "ds_read_b128 v[72:75], v10 offset:20480\n\t"
      "v_accvgpr_write_b32 a144, v150\n\t"
      "v_mfma_f32_16x16x32_bf16 v[92:95], v[48:51], a[28:31], v[92:95]\n\t"
      "buffer_load_dwordx4 v8, s[40:43], s67 offen nt lds\n\t"
      "v_mfma_f32_16x16x32_bf16 v[96:99], v[48:51], a[32:35], v[96:99]\n\t"
      "s_add_u32 m0, s75, 0x15780\n\t"
      "ds_read_b128 v[76:79], v11 offset:20480\n\t"
      "v_mfma_f32_16x16x32_bf16 v[112:115], a[136:139], a[88:91], 0\n\t"
      "v_accvgpr_write_b32 a145, v151\n\t"
      "v_accvgpr_write_b32 a160, v154\n\t"
      "v_mfma_f32_16x16x32_bf16 v[116:119], a[136:139], a[92:95], 0\n\t"
      "s_waitcnt lgkmcnt(4)\n\t"
      "ds_read_b128 v[80:83], v10 offset:22528\n\t"
      "v_mfma_f32_16x16x32_bf16 v[120:123], a[152:155], a[92:95], 0\n\t"
      "ds_read_b128 v[84:87], v11 offset:22528\n\t"
      "v_accvgpr_write_b32 a161, v155\n\t"
      "v_mfma_f32_16x16x32_bf16 v[88:91], v[52:55], a[36:39], v[88:91]\n\t"
      "v_accvgpr_write_b32 a176, v158\n\t"
      "v_accvgpr_write_b32 a177, v159\n\t"
      "v_mfma_f32_16x16x32_bf16 v[92:95], v[52:55], a[40:43], v[92:95]\n\t"
      "buffer_load_dwordx4 v8, s[40:43], s67 offen offset:128 nt lds\n\t"
      "v_mfma_f32_16x16x32_bf16 v[96:99], v[52:55], a[44:47], v[96:99]\n\t"
      "s_add_u32 m0, s75, 0x15f00\n\t"
      "s_nop 0\n\t"
      "buffer_load_dwordx4 v8, s[40:43], s67 offen offset:256 nt lds\n\t"
      "v_mfma_f32_16x16x32_bf16 v[88:91], v[56:59], a[48:51], v[88:91]\n\t"
      "s_add_u32 m0, s39, 0x1d000\n\t"
      "s_nop 0\n\t"
      "buffer_load_dwordx4 v140, s[44:47], s68 offen nt lds\n\t"
      "v_mfma_f32_16x16x32_bf16 v[92:95], v[56:59], a[52:55], v[92:95]\n\t"
      "s_add_u32 s82, s70, 6\n\ts_mul_i32 s68, s82, 0xa800\n\t"
      "s_add_u32 m0, s39, 0x1c000\n\t"
      "v_mfma_f32_16x16x32_bf16 v[96:99], v[56:59], a[56:59], v[96:99]\n\t"
      "buffer_load_dwordx4 v140, s[44:47], s68 offen nt lds\n\t"
      "v_mfma_f32_16x16x32_bf16 v[88:91], v[60:63], a[60:63], v[88:91]\n\t"
      "s_add_u32 s82, s70, 7\n\ts_cmp_lt_u32 s82, 0x70\n\ts_cselect_b32 s46, 0x498000, 0\n\ts_min_i32 s82, s82, 0x6f\n\ts_mul_i32 s68, s82, 0xa800\n\t"
      "v_mfma_f32_16x16x32_bf16 v[92:95], v[60:63], a[64:67], v[92:95]\n\t"
      "s_add_u32 m0, s39, 0x1b000\n\t"
      "s_nop 0\n\t"
      "buffer_load_dwordx4 v140, s[44:47], s68 offen nt lds\n\t"
      "v_mfma_f32_16x16x32_bf16 v[96:99], v[60:63], a[68:71], v[96:99]\n\t"
      "s_waitcnt vmcnt(10) lgkmcnt(0)\n\t"
      "s_barrier\n\t"
      "s_add_u32 m0, s75, 0x24c00\n\t"
      "v_mfma_f32_16x16x32_bf16 v[100:103], v[64:67], a[0:3], v[112:115]\n\t"
      "s_add_u32 s81, s79, 8\n\ts_mul_i32 s67, s81, 0x15000\n\t"
      "v_cvt_pk_bf16_f32 v148, v88, v89\n\t"
      "v_mfma_f32_16x16x32_bf16 v[104:107], v[64:67], a[4:7], v[116:119]\n\t"
      "ds_read_b128 v[40:43], v10 offset:24576\n\t"
      "v_cvt_pk_bf16_f32 v149, v90, v91\n\t"
      "v_mfma_f32_16x16x32_bf16 v[108:111], v[64:67], a[8:11], v[120:123]\n\t"
      "v_cvt_pk_bf16_f32 v152, v92, v93\n\t"
      "v_cvt_pk_bf16_f32 v153, v94, v95\n\t"
      "v_mfma_f32_16x16x32_bf16 v[100:103], v[68:71], a[12:15], v[100:103]\n\t"
      "ds_read_b128 v[44:47], v11 offset:24576\n\t"
      "v_cvt_pk_bf16_f32 v156, v96, v97\n\t"
      "v_mfma_f32_16x16x32_bf16 v[200:203], v[148:151], a[80:83], 0\n\t"
      "v_cvt_pk_bf16_f32 v157, v98, v99\n\t"
      "v_accvgpr_write_b32 a98, v148\n\t"
      "v_mfma_f32_16x16x32_bf16 v[204:207], v[152:155], a[80:83], 0\n\t"
      "ds_read_b128 v[48:51], v10 offset:26624\n\t"
      "v_accvgpr_write_b32 a99, v149\n\t"
      "v_mfma_f32_16x16x32_bf16 v[208:211], v[156:159], a[80:83], 0\n\t"
      "v_accvgpr_write_b32 a114, v152\n\t"
      "v_accvgpr_write_b32 a115, v153\n\t"
      "v_mfma_f32_16x16x32_bf16 v[104:107], v[68:71], a[16:19], v[104:107]\n\t"
      "ds_read_b128 a[168:171], v141 offset:20480\n\t"
      "v_cvt_pk_bf16_f32 v200, v200, v201\n\t"
      "v_mfma_f32_16x16x32_bf16 v[108:111], v[68:71], a[20:23], v[108:111]\n\t"
      "v_cvt_pk_bf16_f32 v201, v202, v203\n\t"
      "v_cvt_pk_bf16_f32 v204, v204, v205\n\t"
      "v_mfma_f32_16x16x32_bf16 v[100:103], v[72:75], a[24:27], v[100:103]\n\t"
      "ds_read_b128 a[184:187], v142 offset:20480\n\t"
      "v_cvt_pk_bf16_f32 v205, v206, v207\n\t"
      "v_mfma_f32_16x16x32_bf16 v[104:107], v[72:75], a[28:31], v[104:107]\n\t"
      "ds_write_b64 v13, v[200:201]\n\t"
      "v_mfma_f32_16x16x32_bf16 v[108:111], v[72:75], a[32:35], v[108:111]\n\t"
      "v_cvt_pk_bf16_f32 v208, v208, v209\n\t"
      "v_cvt_pk_bf16_f32 v209, v210, v211\n\t"
      "v_mfma_f32_16x16x32_bf16 v[100:103], v[76:79], a[36:39], v[100:103]\n\t"
      "ds_read_b128 v[52:55], v11 offset:26624\n\t"
      "v_accvgpr_write_b32 a130, v156\n\t"
      "v_mfma_f32_16x16x32_bf16 v[104:107], v[76:79], a[40:43], v[104:107]\n\t"
      "ds_write_b64 v13, v[204:205] offset:8\n\t"
      "v_mfma_f32_16x16x32_bf16 v[212:215], v[148:151], a[72:75], 0\n\t"
      "v_accvgpr_write_b32 a131, v157\n\t"
      "v_mfma_f32_16x16x32_bf16 v[216:219], v[152:155], a[72:75], 0\n\t"
      "ds_read_b128 v[56:59], v10 offset:28672\n\t"
      "v_mfma_f32_16x16x32_bf16 v[220:223], v[156:159], a[72:75], 0\n\t"
      "buffer_load_dwordx4 v8, s[40:43], s67 offen nt lds\n\t"
      "v_mfma_f32_16x16x32_bf16 v[108:111], v[76:79], a[44:47], v[108:111]\n\t"
      "s_add_u32 m0, s75, 0x25380\n\t"
      "s_nop 0\n\t"
      "buffer_load_dwordx4 v8, s[40:43], s67 offen offset:128 nt lds\n\t"
      "v_mfma_f32_16x16x32_bf16 v[100:103], v[80:83], a[48:51], v[100:103]\n\t"
      "s_add_u32 m0, s75, 0x25b00\n\t"
      "s_nop 0\n\t"
      "buffer_load_dwordx4 v8, s[40:43], s67 offen offset:256 nt lds\n\t"
      "v_mfma_f32_16x16x32_bf16 v[104:107], v[80:83], a[52:55], v[104:107]\n\t"
      "s_add_u32 s81, s79, 10\n\ts_mul_i32 s67, s81, 0x15000\n\t"
      "s_add_u32 m0, s75, 0x18000\n\t"
      "v_mfma_f32_16x16x32_bf16 v[108:111], v[80:83], a[56:59], v[108:111]\n\t"
      "buffer_load_dwordx4 v8, s[40:43], s67 offen nt lds\n\t"
      "v_mfma_f32_16x16x32_bf16 v[100:103], v[84:87], a[60:63], v[100:103]\n\t"
      "s_add_u32 m0, s75, 0x18780\n\t"
      "s_nop 0\n\t"
      "buffer_load_dwordx4 v8, s[40:43], s67 offen offset:128 nt lds\n\t"
      "v_mfma_f32_16x16x32_bf16 v[104:107], v[84:87], a[64:67], v[104:107]\n\t"
      "s_add_u32 m0, s75, 0x18f00\n\t"
      "s_nop 0\n\t"
      "buffer_load_dwordx4 v8, s[40:43], s67 offen offset:256 nt lds\n\t"
      "v_mfma_f32_16x16x32_bf16 v[108:111], v[84:87], a[68:71], v[108:111]\n\t"
      "s_waitcnt lgkmcnt(1)\n\t"
      "s_add_u32 s81, s79, 12\n\ts_mul_i32 s67, s81, 0x15000\n\t"
      "v_mfma_f32_16x16x32_bf16 v[88:91], v[40:43], a[0:3], v[112:115]\n\t"
      "s_add_u32 m0, s75, 0x21000\n\t"
      "ds_read_b128 v[60:63], v11 offset:28672\n\t"
      "v_mfma_f32_16x16x32_bf16 v[92:95], v[40:43], a[4:7], v[116:119]\n\t"
      "ds_write_b64 v20, v[208:209]\n\t"
      "v_mfma_f32_16x16x32_bf16 v[96:99], v[40:43], a[8:11], v[120:123]\n\t"
      "v_cvt_pk_bf16_f32 v162, v100, v101\n\t"
      "v_cvt_pk_bf16_f32 v163, v102, v103\n\t"
      "v_mfma_f32_16x16x32_bf16 v[224:227], v[148:151], a[84:87], 0\n\t"
      "ds_read_b128 v[64:67], v10 offset:30720\n\t"
      "v_cvt_pk_bf16_f32 v166, v104, v105\n\t"
      "v_mfma_f32_16x16x32_bf16 v[228:231], v[152:155], a[84:87], 0\n\t"
      "v_cvt_pk_bf16_f32 v167, v106, v107\n\t"
      "v_cvt_pk_bf16_f32 v170, v108, v109\n\t"
      "v_mfma_f32_16x16x32_bf16 v[232:235], v[156:159], a[84:87], 0\n\t"
      "ds_read_b128 v[68:71], v11 offset:30720\n\t"
      "v_cvt_pk_bf16_f32 v171, v110, v111\n\t"
      "v_mfma_f32_16x16x32_bf16 v[88:91], v[44:47], a[12:15], v[88:91]\n\t"
      "v_cvt_pk_bf16_f32 v212, v212, v213\n\t"
      "v_cvt_pk_bf16_f32 v213, v214, v215\n\t"
      "v_mfma_f32_16x16x32_bf16 v[92:95], v[44:47], a[16:19], v[92:95]\n\t"
      "ds_read_b128 v[72:75], v10 offset:32768\n\t"
      "v_cvt_pk_bf16_f32 v216, v216, v217\n\t"
      "v_mfma_f32_16x16x32_bf16 v[96:99], v[44:47], a[20:23], v[96:99]\n\t"
      "ds_write_b64 v13, v[212:213] offset:12288\n\t"
      "v_mfma_f32_16x16x32_bf16 v[88:91], v[48:51], a[24:27], v[88:91]\n\t"
      "v_cvt_pk_bf16_f32 v217, v218, v219\n\t"
      "v_cvt_pk_bf16_f32 v220, v220, v221\n\t"
      "v_mfma_f32_16x16x32_bf16 v[92:95], v[48:51], a[28:31], v[92:95]\n\t"
      "ds_read_b128 v[76:79], v11 offset:32768\n\t"
      "v_cvt_pk_bf16_f32 v221, v222, v223\n\t"
      "v_mfma_f32_16x16x32_bf16 v[96:99], v[48:51], a[32:35], v[96:99]\n\t"
      "ds_write_b64 v13, v[216:217] offset:12296\n\t"
      "v_mfma_f32_16x16x32_bf16 v[124:127], a[168:171], a[88:91], 0\n\t"
      "v_cvt_pk_bf16_f32 v224, v224, v225\n\t"
      "v_cvt_pk_bf16_f32 v225, v226, v227\n\t"
      "v_mfma_f32_16x16x32_bf16 v[128:131], a[168:171], a[92:95], 0\n\t"
      "ds_read_b128 v[80:83], v10 offset:34816\n\t"
      "v_cvt_pk_bf16_f32 v228, v228, v229\n\t"
      "v_mfma_f32_16x16x32_bf16 v[132:135], a[184:187], a[92:95], 0\n\t"
      "ds_write_b64 v20, v[220:221] offset:12288\n\t"
      "v_mfma_f32_16x16x32_bf16 v[200:203], v[148:151], a[76:79], 0\n\t"
      "v_cvt_pk_bf16_f32 v229, v230, v231\n\t"
      "v_cvt_pk_bf16_f32 v232, v232, v233\n\t"
      "v_mfma_f32_16x16x32_bf16 v[204:207], v[152:155], a[76:79], 0\n\t"
      "ds_read_b128 v[84:87], v11 offset:34816\n\t"
      "v_cvt_pk_bf16_f32 v233, v234, v235\n\t"
      "v_mfma_f32_16x16x32_bf16 v[208:211], v[156:159], a[76:79], 0\n\t"
      "ds_write_b64 v13, v[224:225] offset:24576\n\t"
      "v_mfma_f32_16x16x32_bf16 v[88:91], v[52:55], a[36:39], v[88:91]\n\t"
      "v_cvt_pk_bf16_f32 v200, v200, v201\n\t"
      "v_cvt_pk_bf16_f32 v201, v202, v203\n\t"
      "v_mfma_f32_16x16x32_bf16 v[92:95], v[52:55], a[40:43], v[92:95]\n\t"
      "v_cvt_pk_bf16_f32 v204, v204, v205\n\t"
      "v_cvt_pk_bf16_f32 v205, v206, v207\n\t"
      "v_mfma_f32_16x16x32_bf16 v[96:99], v[52:55], a[44:47], v[96:99]\n\t"
      "s_waitcnt lgkmcnt(5)\n\t"
      "v_cvt_pk_bf16_f32 v208, v208, v209\n\t"
      "v_cvt_pk_bf16_f32 v209, v210, v211\n\t"
      "v_mfma_f32_16x16x32_bf16 v[88:91], v[56:59], a[48:51], v[88:91]\n\t"
      "ds_write_b64 v13, v[228:229] offset:24584\n\t"
      "v_mfma_f32_16x16x32_bf16 v[92:95], v[56:59], a[52:55], v[92:95]\n\t"
      "v_accvgpr_write_b32 a146, v162\n\t"
      "v_accvgpr_write_b32 a147, v163\n\t"
      "v_mfma_f32_16x16x32_bf16 v[96:99], v[56:59], a[56:59], v[96:99]\n\t"
      "v_accvgpr_write_b32 a162, v166\n\t"
      "v_accvgpr_write_b32 a163, v167\n\t"
      "v_mfma_f32_16x16x32_bf16 v[88:91], v[60:63], a[60:63], v[88:91]\n\t"
      "ds_write_b64 v20, v[232:233] offset:24576\n\t"
      "v_mfma_f32_16x16x32_bf16 v[92:95], v[60:63], a[64:67], v[92:95]\n\t"
      "v_accvgpr_write_b32 a178, v170\n\t"
      "v_accvgpr_write_b32 a179, v171\n\t"
      "v_mfma_f32_16x16x32_bf16 v[96:99], v[60:63], a[68:71], v[96:99]\n\t"
      "s_waitcnt vmcnt(9) lgkmcnt(3)\n\t"
      "s_barrier\n\t"
      "v_mfma_f32_16x16x32_bf16 v[100:103], v[64:67], a[0:3], v[124:127]\n\t"
      "ds_read_b128 v[40:43], v10 offset:36864\n\t"
      "v_cvt_pk_bf16_f32 v160, v88, v89\n\t"
      "v_mfma_f32_16x16x32_bf16 v[104:107], v[64:67], a[4:7], v[128:131]\n\t"
      "v_cvt_pk_bf16_f32 v161, v90, v91\n\t"
      "v_cvt_pk_bf16_f32 v164, v92, v93\n\t"
      "v_mfma_f32_16x16x32_bf16 v[108:111], v[64:67], a[8:11], v[132:135]\n\t"
      "ds_read_b128 v[44:47], v11 offset:36864\n\t"
      "v_cvt_pk_bf16_f32 v165, v94, v95\n\t"
      "v_mfma_f32_16x16x32_bf16 v[100:103], v[68:71], a[12:15], v[100:103]\n\t"
      "v_cvt_pk_bf16_f32 v168, v96, v97\n\t"
      "v_cvt_pk_bf16_f32 v169, v98, v99\n\t"
      "v_mfma_f32_16x16x32_bf16 v[212:215], v[160:163], a[80:83], 0\n\t"
      "ds_read_b128 v[48:51], v10 offset:38912\n\t"
      "v_accvgpr_write_b32 a100, v160\n\t"
      "v_mfma_f32_16x16x32_bf16 v[216:219], v[164:167], a[80:83], 0\n\t"
      "ds_write_b64 v13, v[200:201] offset:36864\n\t"
      "v_mfma_f32_16x16x32_bf16 v[220:223], v[168:171], a[80:83], 0\n\t"
      "buffer_store_dwordx2 v[248:249], v253, s[52:55], s38 offen sc1\n\t"
      "buffer_load_dwordx4 v8, s[40:43], s67 offen nt lds\n\t"
      "v_mfma_f32_16x16x32_bf16 v[104:107], v[68:71], a[16:19], v[104:107]\n\t"
      "s_add_u32 m0, s75, 0x21780\n\t"
      "s_nop 0\n\t"
      "buffer_load_dwordx4 v8, s[40:43], s67 offen offset:128 nt lds\n\t"
      "v_mfma_f32_16x16x32_bf16 v[108:111], v[68:71], a[20:23], v[108:111]\n\t"
      "s_add_u32 m0, s75, 0x21f00\n\t"
      "s_nop 0\n\t"
      "buffer_load_dwordx4 v8, s[40:43], s67 offen offset:256 nt lds\n\t"
      "v_mfma_f32_16x16x32_bf16 v[100:103], v[72:75], a[24:27], v[100:103]\n\t"
      "s_add_u32 s81, s79, 14\n\ts_cmp_lt_u32 s81, 0xe0\n\ts_cselect_b32 s42, 0x1260000, 0\n\ts_min_i32 s81, s81, 0xdf\n\ts_mul_i32 s67, s81, 0x15000\n\t"
      "v_mfma_f32_16x16x32_bf16 v[104:107], v[72:75], a[28:31], v[104:107]\n\t"
      "s_add_u32 m0, s75, 0x1e000\n\t"
      "ds_read_b128 a[108:111], v141 offset:16384\n\t"
      "v_mfma_f32_16x16x32_bf16 v[108:111], v[72:75], a[32:35], v[108:111]\n\t"
      "ds_write_b64 v13, v[204:205] offset:36872\n\t"
      "v_mfma_f32_16x16x32_bf16 v[100:103], v[76:79], a[36:39], v[100:103]\n\t"
      "v_cvt_pk_bf16_f32 v212, v212, v213\n\t"
      "v_cvt_pk_bf16_f32 v213, v214, v215\n\t"
      "v_mfma_f32_16x16x32_bf16 v[104:107], v[76:79], a[40:43], v[104:107]\n\t"
      "ds_read_b128 a[124:127], v142 offset:16384\n\t"
      "v_cvt_pk_bf16_f32 v216, v216, v217\n\t"
      "v_mfma_f32_16x16x32_bf16 v[224:227], v[160:163], a[72:75], 0\n\t"
      "ds_write_b64 v20, v[208:209] offset:36864\n\t"
      "v_mfma_f32_16x16x32_bf16 v[228:231], v[164:167], a[72:75], 0\n\t"
      "v_cvt_pk_bf16_f32 v217, v218, v219\n\t"
      "v_cvt_pk_bf16_f32 v220, v220, v221\n\t"
      "v_mfma_f32_16x16x32_bf16 v[232:235], v[168:171], a[72:75], 0\n\t"
      "ds_read_b128 v[52:55], v11 offset:38912\n\t"
      "v_cvt_pk_bf16_f32 v221, v222, v223\n\t"
      "v_mfma_f32_16x16x32_bf16 v[108:111], v[76:79], a[44:47], v[108:111]\n\t"
      "ds_write_b64 v14, v[212:213]\n\t"
      "v_mfma_f32_16x16x32_bf16 v[100:103], v[80:83], a[48:51], v[100:103]\n\t"
      "v_cvt_pk_bf16_f32 v224, v224, v225\n\t"
      "v_cvt_pk_bf16_f32 v225, v226, v227\n\t"
      "v_mfma_f32_16x16x32_bf16 v[104:107], v[80:83], a[52:55], v[104:107]\n\t"
      "ds_read_b128 v[56:59], v10 offset:40960\n\t"
      "v_cvt_pk_bf16_f32 v228, v228, v229\n\t"
      "v_mfma_f32_16x16x32_bf16 v[108:111], v[80:83], a[56:59], v[108:111]\n\t"
      "ds_write_b64 v14, v[216:217] offset:8\n\t"
      "v_mfma_f32_16x16x32_bf16 v[100:103], v[84:87], a[60:63], v[100:103]\n\t"
      "v_cvt_pk_bf16_f32 v229, v230, v231\n\t"
      "v_cvt_pk_bf16_f32 v232, v232, v233\n\t"
      "v_mfma_f32_16x16x32_bf16 v[104:107], v[84:87], a[64:67], v[104:107]\n\t"
      "ds_read_b128 v[60:63], v11 offset:40960\n\t"
      "v_cvt_pk_bf16_f32 v233, v234, v235\n\t"
      "v_mfma_f32_16x16x32_bf16 v[108:111], v[84:87], a[68:71], v[108:111]\n\t"
      "s_waitcnt lgkmcnt(6)\n\t"
      "s_nop 0\n\t"
      "v_cvt_pk_bf16_f32 v174, v100, v101\n\t"
      "v_cvt_pk_bf16_f32 v175, v102, v103\n\t"
      "v_mfma_f32_16x16x32_bf16 v[88:91], v[40:43], a[0:3], v[124:127]\n\t"
      "ds_read_b128 a[140:143], v141 offset:12288\n\t"
      "v_cvt_pk_bf16_f32 v178, v104, v105\n\t"
      "v_mfma_f32_16x16x32_bf16 v[92:95], v[40:43], a[4:7], v[128:131]\n\t"
      "ds_write_b64 v21, v[220:221]\n\t"
      "v_mfma_f32_16x16x32_bf16 v[96:99], v[40:43], a[8:11], v[132:135]\n\t"
      "v_cvt_pk_bf16_f32 v179, v106, v107\n\t"
      "v_cvt_pk_bf16_f32 v182, v108, v109\n\t"
      "v_mfma_f32_16x16x32_bf16 v[200:203], v[160:163], a[84:87], 0\n\t"
      "ds_read_b128 a[156:159], v142 offset:12288\n\t"
      "v_cvt_pk_bf16_f32 v183, v110, v111\n\t"
      "v_mfma_f32_16x16x32_bf16 v[204:207], v[164:167], a[84:87], 0\n\t"
      "ds_write_b64 v14, v[224:225] offset:12288\n\t"
      "v_mfma_f32_16x16x32_bf16 v[208:211], v[168:171], a[84:87], 0\n\t"
      "v_accvgpr_write_b32 a101, v161\n\t"
      "v_accvgpr_write_b32 a116, v164\n\t"
      "v_mfma_f32_16x16x32_bf16 v[88:91], v[44:47], a[12:15], v[88:91]\n\t"
      "ds_read_b128 v[64:67], v10 offset:43008\n\t"
      "v_accvgpr_write_b32 a117, v165\n\t"
      "v_mfma_f32_16x16x32_bf16 v[92:95], v[44:47], a[16:19], v[92:95]\n\t"
      "ds_write_b64 v14, v[228:229] offset:12296\n\t"
      "v_mfma_f32_16x16x32_bf16 v[96:99], v[44:47], a[20:23], v[96:99]\n\t"
      "v_accvgpr_write_b32 a132, v168\n\t"
      "v_accvgpr_write_b32 a133, v169\n\t"
      "v_mfma_f32_16x16x32_bf16 v[88:91], v[48:51], a[24:27], v[88:91]\n\t"
      "ds_read_b128 v[68:71], v11 offset:43008\n\t"
      "v_cvt_pk_bf16_f32 v200, v200, v201\n\t"
      "v_mfma_f32_16x16x32_bf16 v[92:95], v[48:51], a[28:31], v[92:95]\n\t"
      "ds_write_b64 v21, v[232:233] offset:12288\n\t"
      "v_mfma_f32_16x16x32_bf16 v[96:99], v[48:51], a[32:35], v[96:99]\n\t"
      "v_cvt_pk_bf16_f32 v201, v202, v203\n\t"
      "v_cvt_pk_bf16_f32 v204, v204, v205\n\t"
      "v_mfma_f32_16x16x32_bf16 v[112:115], a[108:111], a[88:91], 0\n\t"
      "ds_read_b128 v[72:75], v10 offset:45056\n\t"
      "v_cvt_pk_bf16_f32 v205, v206, v207\n\t"
      "v_mfma_f32_16x16x32_bf16 v[116:119], a[108:111], a[92:95], 0\n\t"
      "ds_write_b64 v14, v[200:201] offset:24576\n\t"
      "v_mfma_f32_16x16x32_bf16 v[120:123], a[124:127], a[92:95], 0\n\t"
      "v_cvt_pk_bf16_f32 v208, v208, v209\n\t"
      "v_cvt_pk_bf16_f32 v209, v210, v211\n\t"
      "v_mfma_f32_16x16x32_bf16 v[212:215], v[160:163], a[76:79], 0\n\t"
      "ds_read_b128 v[76:79], v11 offset:45056\n\t"
      "v_accvgpr_write_b32 a148, v174\n\t"
      "v_mfma_f32_16x16x32_bf16 v[216:219], v[164:167], a[76:79], 0\n\t"
      "ds_write_b64 v14, v[204:205] offset:24584\n\t"
      "v_mfma_f32_16x16x32_bf16 v[220:223], v[168:171], a[76:79], 0\n\t"
      "s_waitcnt lgkmcnt(6)\n\t"
      "ds_read_b128 v[80:83], v10 offset:47104\n\t"
      "v_mfma_f32_16x16x32_bf16 v[88:91], v[52:55], a[36:39], v[88:91]\n\t"
      "s_mul_i32 s39, s80, 0x9000\n\ts_mul_i32 s60, s56, 0x2400\n\ts_add_u32 s39, s39, s60\n\t"
      "v_mfma_f32_16x16x32_bf16 v[92:95], v[52:55], a[40:43], v[92:95]\n\t"
      "ds_read_b128 v[84:87], v11 offset:47104\n\t"
      "v_add_u32_e32 v10, 0xc000, v10\n\t"
      "v_mfma_f32_16x16x32_bf16 v[96:99], v[52:55], a[44:47], v[96:99]\n\t"
      "v_add_u32_e32 v11, 0xc000, v11\n\t"
      "v_cvt_pk_bf16_f32 v212, v212, v213\n\t"
      "v_mfma_f32_16x16x32_bf16 v[88:91], v[56:59], a[48:51], v[88:91]\n\t"
      "ds_write_b64 v21, v[208:209] offset:24576\n\t"
      "v_mfma_f32_16x16x32_bf16 v[92:95], v[56:59], a[52:55], v[92:95]\n\t"
      "v_cvt_pk_bf16_f32 v213, v214, v215\n\t"
      "v_cvt_pk_bf16_f32 v216, v216, v217\n\t"
      "v_mfma_f32_16x16x32_bf16 v[96:99], v[56:59], a[56:59], v[96:99]\n\t"
      "v_cvt_pk_bf16_f32 v217, v218, v219\n\t"
      "v_cvt_pk_bf16_f32 v220, v220, v221\n\t"
      "v_mfma_f32_16x16x32_bf16 v[88:91], v[60:63], a[60:63], v[88:91]\n\t"
      "ds_write_b64 v14, v[212:213] offset:36864\n\t"
      "v_mfma_f32_16x16x32_bf16 v[92:95], v[60:63], a[64:67], v[92:95]\n\t"
      "v_cvt_pk_bf16_f32 v221, v222, v223\n\t"
      "v_accvgpr_write_b32 a149, v175\n\t"
      "v_mfma_f32_16x16x32_bf16 v[96:99], v[60:63], a[68:71], v[96:99]\n\t"
      "s_nop 2\n\t"
      "v_cvt_pk_bf16_f32 v172, v88, v89\n\t"
      "s_waitcnt vmcnt(7) lgkmcnt(2)\n\t"
      "v_cvt_pk_bf16_f32 v173, v90, v91\n\t"
      "v_accvgpr_write_b32 a164, v178\n\t"
      "s_barrier\n\t"
      "v_mfma_f32_16x16x32_bf16 v[100:103], v[64:67], a[0:3], v[112:115]\n\t"
      "buffer_load_dwordx4 v8, s[40:43], s67 offen nt lds\n\t"
      "v_mfma_f32_16x16x32_bf16 v[104:107], v[64:67], a[4:7], v[116:119]\n\t"
      "s_add_u32 m0, s75, 0x1e780\n\t"
      "ds_read_b128 v[40:43], v10 offset:52224\n\t"
      "v_mfma_f32_16x16x32_bf16 v[108:111], v[64:67], a[8:11], v[120:123]\n\t"
      "v_cvt_pk_bf16_f32 v176, v92, v93\n\t"
      "v_cvt_pk_bf16_f32 v177, v94, v95\n\t"
      "v_mfma_f32_16x16x32_bf16 v[100:103], v[68:71], a[12:15], v[100:103]\n\t"
      "ds_read_b128 v[44:47], v11 offset:52224\n\t"
      "v_cvt_pk_bf16_f32 v180, v96, v97\n\t"
      "v_mfma_f32_16x16x32_bf16 v[224:227], v[172:175], a[80:83], 0\n\t"
      "v_cvt_pk_bf16_f32 v181, v98, v99\n\t"
      "v_accvgpr_write_b32 a165, v179\n\t"
      "v_mfma_f32_16x16x32_bf16 v[228:231], v[176:179], a[80:83], 0\n\t"
      "ds_read_b128 v[48:51], v10 offset:54272\n\t"
      "v_accvgpr_write_b32 a180, v182\n\t"
      "v_mfma_f32_16x16x32_bf16 v[232:235], v[180:183], a[80:83], 0\n\t"
      "ds_write_b64 v14, v[216:217] offset:36872\n\t"
      "v_mfma_f32_16x16x32_bf16 v[104:107], v[68:71], a[16:19], v[104:107]\n\t"
      "buffer_load_dwordx4 v8, s[40:43], s67 offen offset:128 nt lds\n\t"
      "v_mfma_f32_16x16x32_bf16 v[108:111], v[68:71], a[20:23], v[108:111]\n\t"
      "s_add_u32 m0, s75, 0x1ef00\n\t"
      "ds_read_b128 v[52:55], v11 offset:54272\n\t"
      "v_mfma_f32_16x16x32_bf16 v[100:103], v[72:75], a[24:27], v[100:103]\n\t"
      "ds_write_b64 v21, v[220:221] offset:36864\n\t"
      "v_mfma_f32_16x16x32_bf16 v[104:107], v[72:75], a[28:31], v[104:107]\n\t"
      "v_accvgpr_write_b32 a181, v183\n\t"
      "v_cvt_pk_bf16_f32 v224, v224, v225\n\t"
      "v_mfma_f32_16x16x32_bf16 v[108:111], v[72:75], a[32:35], v[108:111]\n\t"
      "ds_read_b128 v[56:59], v10 offset:56320\n\t"
      "v_cvt_pk_bf16_f32 v225, v226, v227\n\t"
      "v_mfma_f32_16x16x32_bf16 v[100:103], v[76:79], a[36:39], v[100:103]\n\t"
      "ds_write_b64 v15, v[224:225]\n\t"
      "v_mfma_f32_16x16x32_bf16 v[104:107], v[76:79], a[40:43], v[104:107]\n\t"
      "buffer_load_dwordx4 v8, s[40:43], s67 offen offset:256 nt lds\n\t"
      "v_mfma_f32_16x16x32_bf16 v[200:203], v[172:175], a[72:75], 0\n\t"
      "s_mov_b32 m0, 0x27c00\n\t"
      "ds_read_b128 v[60:63], v11 offset:56320\n\t"
      "v_mfma_f32_16x16x32_bf16 v[204:207], v[176:179], a[72:75], 0\n\t"
      "v_cvt_pk_bf16_f32 v228, v228, v229\n\t"
      "v_cvt_pk_bf16_f32 v229, v230, v231\n\t"
      "v_mfma_f32_16x16x32_bf16 v[208:211], v[180:183], a[72:75], 0\n\t"
      "ds_read_b128 a[172:175], v141 offset:8192\n\t"
      "v_cvt_pk_bf16_f32 v232, v232, v233\n\t"
      "v_mfma_f32_16x16x32_bf16 v[108:111], v[76:79], a[44:47], v[108:111]\n\t"
      "ds_write_b64 v15, v[228:229] offset:8\n\t"
      "v_mfma_f32_16x16x32_bf16 v[100:103], v[80:83], a[48:51], v[100:103]\n\t"
      "v_cvt_pk_bf16_f32 v233, v234, v235\n\t"
      "v_cvt_pk_bf16_f32 v200, v200, v201\n\t"
      "v_mfma_f32_16x16x32_bf16 v[104:107], v[80:83], a[52:55], v[104:107]\n\t"
      "ds_read_b128 a[188:191], v142 offset:8192\n\t"
      "v_cvt_pk_bf16_f32 v201, v202, v203\n\t"
      "v_mfma_f32_16x16x32_bf16 v[108:111], v[80:83], a[56:59], v[108:111]\n\t"
      "ds_write_b64 v22, v[232:233]\n\t"
      "v_mfma_f32_16x16x32_bf16 v[100:103], v[84:87], a[60:63], v[100:103]\n\t"
      "v_cvt_pk_bf16_f32 v204, v204, v205\n\t"
      "v_cvt_pk_bf16_f32 v205, v206, v207\n\t"
      "v_mfma_f32_16x16x32_bf16 v[104:107], v[84:87], a[64:67], v[104:107]\n\t"
      "ds_read_b128 a[240:243], v141 offset:4096\n\t"
      "v_cvt_pk_bf16_f32 v208, v208, v209\n\t"
      "v_mfma_f32_16x16x32_bf16 v[108:111], v[84:87], a[68:71], v[108:111]\n\t"
      "s_waitcnt lgkmcnt(6)\n\t"
      "s_nop 0\n\t"
      "v_cvt_pk_bf16_f32 v150, v100, v101\n\t"
      "v_cvt_pk_bf16_f32 v151, v102, v103\n\t"
      "v_mfma_f32_16x16x32_bf16 v[88:91], v[40:43], a[0:3], v[112:115]\n\t"
      "ds_read_b128 a[244:247], v142 offset:4096\n\t"
      "v_cvt_pk_bf16_f32 v154, v104, v105\n\t"
      "v_mfma_f32_16x16x32_bf16 v[92:95], v[40:43], a[4:7], v[116:119]\n\t"
      "v_cvt_pk_bf16_f32 v155, v106, v107\n\t"
      "v_cvt_pk_bf16_f32 v158, v108, v109\n\t"
      "v_mfma_f32_16x16x32_bf16 v[96:99], v[40:43], a[8:11], v[120:123]\n\t"
      "ds_read_b128 a[248:251], v141\n\t"
      "v_cvt_pk_bf16_f32 v159, v110, v111\n\t"
      "v_mfma_f32_16x16x32_bf16 v[212:215], v[172:175], a[84:87], 0\n\t"
      "ds_write_b64 v15, v[200:201] offset:12288\n\t"
      "v_mfma_f32_16x16x32_bf16 v[216:219], v[176:179], a[84:87], 0\n\t"
      "v_cvt_pk_bf16_f32 v209, v210, v211\n\t"
      "v_accvgpr_write_b32 a102, v172\n\t"
      "v_mfma_f32_16x16x32_bf16 v[220:223], v[180:183], a[84:87], 0\n\t"
      "ds_read_b128 a[252:255], v142\n\t"
      "v_accvgpr_write_b32 a103, v173\n\t"
      "v_mfma_f32_16x16x32_bf16 v[88:91], v[44:47], a[12:15], v[88:91]\n\t"
      "ds_write_b64 v15, v[204:205] offset:12296\n\t"
      "v_mfma_f32_16x16x32_bf16 v[92:95], v[44:47], a[16:19], v[92:95]\n\t"
      "v_accvgpr_write_b32 a118, v176\n\t"
      "v_accvgpr_write_b32 a119, v177\n\t"
      "v_mfma_f32_16x16x32_bf16 v[96:99], v[44:47], a[20:23], v[96:99]\n\t"
      "ds_read_b128 v[64:67], v10 offset:58368\n\t"
      "v_accvgpr_write_b32 a134, v180\n\t"
      "v_mfma_f32_16x16x32_bf16 v[88:91], v[48:51], a[24:27], v[88:91]\n\t"
      "ds_write_b64 v22, v[208:209] offset:12288\n\t"
      "v_mfma_f32_16x16x32_bf16 v[92:95], v[48:51], a[28:31], v[92:95]\n\t"
      "v_accvgpr_write_b32 a135, v181\n\t"
      "v_cvt_pk_bf16_f32 v212, v212, v213\n\t"
      "v_mfma_f32_16x16x32_bf16 v[96:99], v[48:51], a[32:35], v[96:99]\n\t"
      "ds_read_b128 v[68:71], v11 offset:58368\n\t"
      "v_cvt_pk_bf16_f32 v213, v214, v215\n\t"
      "v_mfma_f32_16x16x32_bf16 v[124:127], a[140:143], a[88:91], 0\n\t"
      "ds_write_b64 v15, v[212:213] offset:24576\n\t"
      "v_mfma_f32_16x16x32_bf16 v[128:131], a[140:143], a[92:95], 0\n\t"
      "v_cvt_pk_bf16_f32 v216, v216, v217\n\t"
      "v_cvt_pk_bf16_f32 v217, v218, v219\n\t"
      "v_mfma_f32_16x16x32_bf16 v[132:135], a[156:159], a[92:95], 0\n\t"
      "ds_read_b128 v[72:75], v10 offset:60416\n\t"
      "v_cvt_pk_bf16_f32 v220, v220, v221\n\t"
      "v_mfma_f32_16x16x32_bf16 v[224:227], v[172:175], a[76:79], 0\n\t"
      "ds_write_b64 v15, v[216:217] offset:24584\n\t"
      "v_mfma_f32_16x16x32_bf16 v[228:231], v[176:179], a[76:79], 0\n\t"
      "v_cvt_pk_bf16_f32 v221, v222, v223\n\t"
      "v_accvgpr_write_b32 a150, v150\n\t"
      "v_mfma_f32_16x16x32_bf16 v[232:235], v[180:183], a[76:79], 0\n\t"
      "ds_read_b128 v[76:79], v11 offset:60416\n\t"
      "ds_read_b128 v[80:83], v10 offset:62464\n\t"
      "v_mfma_f32_16x16x32_bf16 v[88:91], v[52:55], a[36:39], v[88:91]\n\t"
      "ds_read_b128 v[84:87], v11 offset:62464\n\t"
      "v_cvt_pk_bf16_f32 v224, v224, v225\n\t"
      "v_mfma_f32_16x16x32_bf16 v[92:95], v[52:55], a[40:43], v[92:95]\n\t"
      "ds_write_b64 v22, v[220:221] offset:24576\n\t"
      "v_mfma_f32_16x16x32_bf16 v[96:99], v[52:55], a[44:47], v[96:99]\n\t"
      "v_cvt_pk_bf16_f32 v225, v226, v227\n\t"
      "v_cvt_pk_bf16_f32 v228, v228, v229\n\t"
      "v_mfma_f32_16x16x32_bf16 v[88:91], v[56:59], a[48:51], v[88:91]\n\t"
      "v_cvt_pk_bf16_f32 v229, v230, v231\n\t"
      "v_cvt_pk_bf16_f32 v232, v232, v233\n\t"
      "v_mfma_f32_16x16x32_bf16 v[92:95], v[56:59], a[52:55], v[92:95]\n\t"
      "ds_write_b64 v15, v[224:225] offset:36864\n\t"
      "v_mfma_f32_16x16x32_bf16 v[96:99], v[56:59], a[56:59], v[96:99]\n\t"
      "s_waitcnt lgkmcnt(7)\n\t"
      "v_cvt_pk_bf16_f32 v233, v234, v235\n\t"
      "v_accvgpr_write_b32 a151, v151\n\t"
      "v_mfma_f32_16x16x32_bf16 v[88:91], v[60:63], a[60:63], v[88:91]\n\t"
      "v_accvgpr_write_b32 a166, v154\n\t"
      "v_accvgpr_write_b32 a167, v155\n\t"
      "v_mfma_f32_16x16x32_bf16 v[92:95], v[60:63], a[64:67], v[92:95]\n\t"
      "ds_write_b64 v15, v[228:229] offset:36872\n\t"
      "v_mfma_f32_16x16x32_bf16 v[96:99], v[60:63], a[68:71], v[96:99]\n\t"
      "s_waitcnt vmcnt(7) lgkmcnt(3)\n\t"
      "s_barrier\n\t"
      "v_accvgpr_write_b32 a182, v158\n\t"
      "v_mfma_f32_16x16x32_bf16 v[100:103], v[64:67], a[0:3], v[124:127]\n\t"
      "buffer_load_dwordx2 v[250:251], v252, s[52:55], s66 offen sc1\n\t"
      "v_cvt_pk_bf16_f32 v148, v88, v89\n\t"
      "v_mfma_f32_16x16x32_bf16 v[104:107], v[64:67], a[4:7], v[128:131]\n\t"
      "ds_read_b128 v[40:43], v10\n\t"
      "v_cvt_pk_bf16_f32 v149, v90, v91\n\t"
      "v_mfma_f32_16x16x32_bf16 v[108:111], v[64:67], a[8:11], v[132:135]\n\t"
      "v_cvt_pk_bf16_f32 v152, v92, v93\n\t"
      "v_cvt_pk_bf16_f32 v153, v94, v95\n\t"
      "v_mfma_f32_16x16x32_bf16 v[100:103], v[68:71], a[12:15], v[100:103]\n\t"
      "ds_read_b128 v[44:47], v11\n\t"
      "v_cvt_pk_bf16_f32 v156, v96, v97\n\t"
      "v_mfma_f32_16x16x32_bf16 v[200:203], v[148:151], a[80:83], 0\n\t"
      "v_cvt_pk_bf16_f32 v157, v98, v99\n\t"
      "v_accvgpr_write_b32 a183, v159\n\t"
      "v_mfma_f32_16x16x32_bf16 v[204:207], v[152:155], a[80:83], 0\n\t"
      "ds_read_b128 v[48:51], v10 offset:2048\n\t"
      "v_accvgpr_write_b32 a104, v148\n\t"
      "v_mfma_f32_16x16x32_bf16 v[208:211], v[156:159], a[80:83], 0\n\t"
      "ds_write_b64 v22, v[232:233] offset:36864\n\t"
      "v_mfma_f32_16x16x32_bf16 v[104:107], v[68:71], a[16:19], v[104:107]\n\t"
      "v_cvt_pk_bf16_f32 v200, v200, v201\n\t"
      "v_cvt_pk_bf16_f32 v201, v202, v203\n\t"
      "v_mfma_f32_16x16x32_bf16 v[108:111], v[68:71], a[20:23], v[108:111]\n\t"
      "ds_read_b128 v[52:55], v11 offset:2048\n\t"
      "v_cvt_pk_bf16_f32 v204, v204, v205\n\t"
      "v_mfma_f32_16x16x32_bf16 v[100:103], v[72:75], a[24:27], v[100:103]\n\t"
      "ds_write_b64 v16, v[200:201]\n\t"
      "v_mfma_f32_16x16x32_bf16 v[104:107], v[72:75], a[28:31], v[104:107]\n\t"
      "v_cvt_pk_bf16_f32 v205, v206, v207\n\t"
      "v_cvt_pk_bf16_f32 v208, v208, v209\n\t"
      "v_mfma_f32_16x16x32_bf16 v[108:111], v[72:75], a[32:35], v[108:111]\n\t"
      "ds_read_b128 v[56:59], v10 offset:4096\n\t"
      "v_cvt_pk_bf16_f32 v209, v210, v211\n\t"
      "v_mfma_f32_16x16x32_bf16 v[100:103], v[76:79], a[36:39], v[100:103]\n\t"
      "ds_write_b64 v16, v[204:205] offset:8\n\t"
      "v_mfma_f32_16x16x32_bf16 v[104:107], v[76:79], a[40:43], v[104:107]\n\t"
      "v_accvgpr_write_b32 a105, v149\n\t"
      "v_accvgpr_write_b32 a120, v152\n\t"
      "v_mfma_f32_16x16x32_bf16 v[212:215], v[148:151], a[72:75], 0\n\t"
      "ds_read_b128 v[60:63], v11 offset:4096\n\t"
      "v_accvgpr_write_b32 a121, v153\n\t"
      "v_mfma_f32_16x16x32_bf16 v[216:219], v[152:155], a[72:75], 0\n\t"
      "ds_write_b64 v23, v[208:209]\n\t"
      "v_mfma_f32_16x16x32_bf16 v[220:223], v[156:159], a[72:75], 0\n\t"
      "v_accvgpr_write_b32 a136, v156\n\t"
      "v_accvgpr_write_b32 a137, v157\n\t"
      "v_mfma_f32_16x16x32_bf16 v[108:111], v[76:79], a[44:47], v[108:111]\n\t"
      "ds_read_b128 v[64:67], v10 offset:6144\n\t"
      "v_cvt_pk_bf16_f32 v212, v212, v213\n\t"
      "v_mfma_f32_16x16x32_bf16 v[100:103], v[80:83], a[48:51], v[100:103]\n\t"
      "v_cvt_pk_bf16_f32 v213, v214, v215\n\t"
      "v_cvt_pk_bf16_f32 v216, v216, v217\n\t"
      "v_mfma_f32_16x16x32_bf16 v[104:107], v[80:83], a[52:55], v[104:107]\n\t"
      "ds_read_b128 v[68:71], v11 offset:6144\n\t"
      "v_cvt_pk_bf16_f32 v217, v218, v219\n\t"
      "v_mfma_f32_16x16x32_bf16 v[108:111], v[80:83], a[56:59], v[108:111]\n\t"
      "ds_write_b64 v16, v[212:213] offset:12288\n\t"
      "v_mfma_f32_16x16x32_bf16 v[100:103], v[84:87], a[60:63], v[100:103]\n\t"
      "v_cvt_pk_bf16_f32 v220, v220, v221\n\t"
      "v_cvt_pk_bf16_f32 v221, v222, v223\n\t"
      "v_mfma_f32_16x16x32_bf16 v[104:107], v[84:87], a[64:67], v[104:107]\n\t"
      "ds_read_b128 v[72:75], v10 offset:8192\n\t"
      "v_mfma_f32_16x16x32_bf16 v[108:111], v[84:87], a[68:71], v[108:111]\n\t"
      "s_waitcnt lgkmcnt(6)\n\t"
      "v_mfma_f32_16x16x32_bf16 v[88:91], v[40:43], a[0:3], v[124:127]\n\t"
      "ds_read_b128 v[76:79], v11 offset:8192\n\t"
      "v_cvt_pk_bf16_f32 v162, v100, v101\n\t"
      "v_mfma_f32_16x16x32_bf16 v[92:95], v[40:43], a[4:7], v[128:131]\n\t"
      "v_cvt_pk_bf16_f32 v163, v102, v103\n\t"
      "v_cvt_pk_bf16_f32 v166, v104, v105\n\t"
      "v_mfma_f32_16x16x32_bf16 v[96:99], v[40:43], a[8:11], v[132:135]\n\t"
      "ds_read_b128 v[80:83], v10 offset:10240\n\t"
      "v_cvt_pk_bf16_f32 v167, v106, v107\n\t"
      "v_mfma_f32_16x16x32_bf16 v[224:227], v[148:151], a[84:87], 0\n\t"
      "v_cvt_pk_bf16_f32 v170, v108, v109\n\t"
      "v_cvt_pk_bf16_f32 v171, v110, v111\n\t"
      "v_mfma_f32_16x16x32_bf16 v[228:231], v[152:155], a[84:87], 0\n\t"
      "ds_read_b128 v[84:87], v11 offset:10240\n\t"
      "v_accvgpr_write_b32 a152, v162\n\t"
      "v_mfma_f32_16x16x32_bf16 v[232:235], v[156:159], a[84:87], 0\n\t"
      "ds_write_b64 v16, v[216:217] offset:12296\n\t"
      "v_mfma_f32_16x16x32_bf16 v[88:91], v[44:47], a[12:15], v[88:91]\n\t"
      "v_cvt_pk_bf16_f32 v224, v224, v225\n\t"
      "v_cvt_pk_bf16_f32 v225, v226, v227\n\t"
      "v_mfma_f32_16x16x32_bf16 v[92:95], v[44:47], a[16:19], v[92:95]\n\t"
      "v_cvt_pk_bf16_f32 v228, v228, v229\n\t"
      "v_cvt_pk_bf16_f32 v229, v230, v231\n\t"
      "v_mfma_f32_16x16x32_bf16 v[96:99], v[44:47], a[20:23], v[96:99]\n\t"
      "ds_write_b64 v23, v[220:221] offset:12288\n\t"
      "v_mfma_f32_16x16x32_bf16 v[88:91], v[48:51], a[24:27], v[88:91]\n\t"
      "v_cvt_pk_bf16_f32 v232, v232, v233\n\t"
      "v_cvt_pk_bf16_f32 v233, v234, v235\n\t"
      "v_mfma_f32_16x16x32_bf16 v[92:95], v[48:51], a[28:31], v[92:95]\n\t"
      "v_accvgpr_write_b32 a153, v163\n\t"
      "v_accvgpr_write_b32 a168, v166\n\t"
      "v_mfma_f32_16x16x32_bf16 v[96:99], v[48:51], a[32:35], v[96:99]\n\t"
      "ds_write_b64 v16, v[224:225] offset:24576\n\t"
      "v_mfma_f32_16x16x32_bf16 v[112:115], a[172:175], a[88:91], 0\n\t"
      "v_accvgpr_write_b32 a169, v167\n\t"
      "v_accvgpr_write_b32 a184, v170\n\t"
      "v_mfma_f32_16x16x32_bf16 v[116:119], a[172:175], a[92:95], 0\n\t"
      "v_accvgpr_write_b32 a185, v171\n\t"
      "v_mfma_f32_16x16x32_bf16 v[120:123], a[188:191], a[92:95], 0\n\t"
      "ds_write_b64 v16, v[228:229] offset:24584\n\t"
      "v_mfma_f32_16x16x32_bf16 v[200:203], v[148:151], a[76:79], 0\n\t"
      "s_cmp_eq_u32 s77, 0\n\ts_cselect_b32 s64, 0, 0x80000000\n\ts_mov_b32 s65, 0x130000\n\t"
      "v_mfma_f32_16x16x32_bf16 v[204:207], v[152:155], a[76:79], 0\n\t"
      "v_mfma_f32_16x16x32_bf16 v[208:211], v[156:159], a[76:79], 0\n\t"
      "ds_write_b64 v23, v[232:233] offset:24576\n\t"
      "v_mfma_f32_16x16x32_bf16 v[88:91], v[52:55], a[36:39], v[88:91]\n\t"
      "v_mfma_f32_16x16x32_bf16 v[92:95], v[52:55], a[40:43], v[92:95]\n\t"
      "v_mfma_f32_16x16x32_bf16 v[96:99], v[52:55], a[44:47], v[96:99]\n\t"
      "v_cvt_pk_bf16_f32 v200, v200, v201\n\t"
      "v_cvt_pk_bf16_f32 v201, v202, v203\n\t"
      "v_mfma_f32_16x16x32_bf16 v[88:91], v[56:59], a[48:51], v[88:91]\n\t"
      "v_cvt_pk_bf16_f32 v204, v204, v205\n\t"
      "v_cvt_pk_bf16_f32 v208, v208, v209\n\t"
      "v_mfma_f32_16x16x32_bf16 v[92:95], v[56:59], a[52:55], v[92:95]\n\t"
      "v_cvt_pk_bf16_f32 v205, v206, v207\n\t"
      "v_cvt_pk_bf16_f32 v209, v210, v211\n\t"
      "v_mfma_f32_16x16x32_bf16 v[96:99], v[56:59], a[56:59], v[96:99]\n\t"
      "s_waitcnt lgkmcnt(2)\n\t"
      "s_mul_i32 s59, s56, 0x300\n\t"
      "s_add_u32 s59, s59, 0x25800\n\t"
      "v_mfma_f32_16x16x32_bf16 v[88:91], v[60:63], a[60:63], v[88:91]\n\t"
      "ds_write_b64 v16, v[200:201] offset:36864\n\t"
      "v_mfma_f32_16x16x32_bf16 v[92:95], v[60:63], a[64:67], v[92:95]\n\t"
      "v_mfma_f32_16x16x32_bf16 v[96:99], v[60:63], a[68:71], v[96:99]\n\t"
      "s_waitcnt vmcnt(0)\n\t"
      "s_mov_b32 s83, 0\n\tLpoll_%=:\n\tv_cmp_ne_u64_e32 vcc, v[248:249], v[250:251]\n\ts_nop 4\n\ts_cbranch_vccz Lok_%=\n\ts_sleep 4\n\tbuffer_load_dwordx2 v[250:251], v252, s[52:55], s66 offen sc1\n\ts_add_u32 s83, s83, 1\n\ts_cmp_lt_u32 s83, 0x8000\n\ts_waitcnt vmcnt(0)\n\ts_cbranch_scc1 Lpoll_%=\n\tLok_%=:\n\ts_nop 1\n\t"
      "s_barrier\n\t"
      "v_mfma_f32_16x16x32_bf16 v[100:103], v[64:67], a[0:3], v[112:115]\n\t"
      "buffer_load_dwordx4 v12, s[52:55], s39 offen sc1 lds\n\t"
      "v_mfma_f32_16x16x32_bf16 v[104:107], v[64:67], a[4:7], v[116:119]\n\t"
      "s_mov_b32 m0, 0x27800\n\t"
      "ds_read_b128 v[40:43], v10 offset:36864\n\t"
      "v_mfma_f32_16x16x32_bf16 v[108:111], v[64:67], a[8:11], v[120:123]\n\t"
      "v_cvt_pk_bf16_f32 v160, v88, v89\n\t"
      "v_cvt_pk_bf16_f32 v161, v90, v91\n\t"
      "v_mfma_f32_16x16x32_bf16 v[100:103], v[68:71], a[12:15], v[100:103]\n\t"
      "v_cvt_pk_bf16_f32 v164, v92, v93\n\t"
      "v_cvt_pk_bf16_f32 v165, v94, v95\n\t"
      "v_mfma_f32_16x16x32_bf16 v[212:215], v[160:163], a[80:83], 0\n\t"
      "buffer_load_dwordx4 v12, s[52:55], s39 offen offset:1024 sc1 lds\n\t"
      "v_mfma_f32_16x16x32_bf16 v[216:219], v[164:167], a[80:83], 0\n\t"
      "s_mov_b32 m0, 0x27400\n\t"
      "v_cvt_pk_bf16_f32 v168, v96, v97\n\t"
      "v_cvt_pk_bf16_f32 v169, v98, v99\n\t"
      "v_accvgpr_write_b32 a106, v160\n\t"
      "v_accvgpr_write_b32 a107, v161\n\t"
      "v_mfma_f32_16x16x32_bf16 v[220:223], v[168:171], a[80:83], 0\n\t"
      "ds_read_b128 v[44:47], v11 offset:36864\n\t"
      "v_accvgpr_write_b32 a122, v164\n\t"
      "v_mfma_f32_16x16x32_bf16 v[104:107], v[68:71], a[16:19], v[104:107]\n\t"
      "ds_write_b64 v16, v[204:205] offset:36872\n\t"
      "v_mfma_f32_16x16x32_bf16 v[108:111], v[68:71], a[20:23], v[108:111]\n\t"
      "buffer_load_dwordx4 v12, s[52:55], s39 offen offset:2048 sc1 lds\n\t"
      "v_mfma_f32_16x16x32_bf16 v[100:103], v[72:75], a[24:27], v[100:103]\n\t"
      "s_add_u32 s39, s39, 0xc00\n\t"
      "s_mov_b32 m0, 0x27c00\n\t"
      "v_accvgpr_write_b32 a123, v165\n\t"
      "v_mfma_f32_16x16x32_bf16 v[104:107], v[72:75], a[28:31], v[104:107]\n\t"
      "ds_read_b128 v[48:51], v10 offset:38912\n\t"
      "v_accvgpr_write_b32 a138, v168\n\t"
      "v_mfma_f32_16x16x32_bf16 v[108:111], v[72:75], a[32:35], v[108:111]\n\t"
      "ds_write_b64 v23, v[208:209] offset:36864\n\t"
      "v_mfma_f32_16x16x32_bf16 v[100:103], v[76:79], a[36:39], v[100:103]\n\t"
      "buffer_load_dwordx4 v12, s[52:55], s39 offen sc1 lds\n\t"
      "v_mfma_f32_16x16x32_bf16 v[104:107], v[76:79], a[40:43], v[104:107]\n\t"
      "s_mov_b32 m0, 0x27800\n\t"
      "ds_read_b128 v[52:55], v11 offset:38912\n\t"
      "v_mfma_f32_16x16x32_bf16 v[224:227], v[160:163], a[72:75], 0\n\t"
      "v_accvgpr_write_b32 a139, v169\n\t"
      "v_cvt_pk_bf16_f32 v212, v212, v213\n\t"
      "v_mfma_f32_16x16x32_bf16 v[228:231], v[164:167], a[72:75], 0\n\t"
      "ds_read_b128 v[56:59], v10 offset:40960\n\t"
      "v_cvt_pk_bf16_f32 v213, v214, v215\n\t"
      "v_mfma_f32_16x16x32_bf16 v[232:235], v[168:171], a[72:75], 0\n\t"
      "buffer_load_dwordx4 v12, s[52:55], s39 offen offset:1024 sc1 lds\n\t"
      "v_mfma_f32_16x16x32_bf16 v[108:111], v[76:79], a[44:47], v[108:111]\n\t"
      "s_mov_b32 m0, 0x27400\n\t"
      "ds_read_b128 v[60:63], v11 offset:40960\n\t"
      "v_mfma_f32_16x16x32_bf16 v[100:103], v[80:83], a[48:51], v[100:103]\n\t"
      "v_or_b32_e32 v250, s64, v253\n\t"
      "v_cvt_pk_bf16_f32 v216, v216, v217\n\t"
      "v_mfma_f32_16x16x32_bf16 v[104:107], v[80:83], a[52:55], v[104:107]\n\t"
      "ds_read_b128 v[64:67], v10 offset:43008\n\t"
      "v_cvt_pk_bf16_f32 v217, v218, v219\n\t"
      "v_mfma_f32_16x16x32_bf16 v[108:111], v[80:83], a[56:59], v[108:111]\n\t"
      "buffer_load_dwordx4 v12, s[52:55], s39 offen offset:2048 sc1 lds\n\t"
      "v_mfma_f32_16x16x32_bf16 v[100:103], v[84:87], a[60:63], v[100:103]\n\t"
      "s_add_u32 s39, s39, 0xc00\n\t"
      "s_mov_b32 m0, 0x27c00\n\t"
      "v_cvt_pk_bf16_f32 v220, v220, v221\n\t"
      "v_mfma_f32_16x16x32_bf16 v[104:107], v[84:87], a[64:67], v[104:107]\n\t"
      "ds_read_b128 v[68:71], v11 offset:43008\n\t"
      "v_cvt_pk_bf16_f32 v221, v222, v223\n\t"
      "v_mfma_f32_16x16x32_bf16 v[108:111], v[84:87], a[68:71], v[108:111]\n\t"
      "s_waitcnt lgkmcnt(5)\n\t"
      "v_cvt_pk_bf16_f32 v174, v100, v101\n\t"
      "v_cvt_pk_bf16_f32 v175, v102, v103\n\t"
      "v_mfma_f32_16x16x32_bf16 v[88:91], v[40:43], a[0:3], v[112:115]\n\t"
      "buffer_load_dwordx4 v12, s[52:55], s39 offen sc1 lds\n\t"
      "v_mfma_f32_16x16x32_bf16 v[92:95], v[40:43], a[4:7], v[116:119]\n\t"
      "s_mov_b32 m0, 0x27800\n\t"
      "ds_read_b128 v[72:75], v10 offset:45056\n\t"
      "v_mfma_f32_16x16x32_bf16 v[96:99], v[40:43], a[8:11], v[120:123]\n\t"
      "v_cvt_pk_bf16_f32 v178, v104, v105\n\t"
      "v_cvt_pk_bf16_f32 v179, v106, v107\n\t"
      "v_mfma_f32_16x16x32_bf16 v[88:91], v[44:47], a[12:15], v[88:91]\n\t"
      "ds_read_b128 v[76:79], v11 offset:45056\n\t"
      "v_cvt_pk_bf16_f32 v182, v108, v109\n\t"
      "v_mfma_f32_16x16x32_bf16 v[92:95], v[44:47], a[16:19], v[92:95]\n\t"
      "buffer_load_dwordx4 v12, s[52:55], s39 offen offset:1024 sc1 lds\n\t"
      "v_mfma_f32_16x16x32_bf16 v[96:99], v[44:47], a[20:23], v[96:99]\n\t"
      "s_mov_b32 m0, 0x27400\n\t"
      "ds_read_b128 v[80:83], v10 offset:47104\n\t"
      "v_mfma_f32_16x16x32_bf16 v[88:91], v[48:51], a[24:27], v[88:91]\n\t"
      "v_cvt_pk_bf16_f32 v183, v110, v111\n\t"
      "v_cvt_pk_bf16_f32 v224, v224, v225\n\t"
      "v_mfma_f32_16x16x32_bf16 v[92:95], v[48:51], a[28:31], v[92:95]\n\t"
      "ds_read_b128 v[84:87], v11 offset:47104\n\t"
      "v_cvt_pk_bf16_f32 v228, v228, v229\n\t"
      "v_mfma_f32_16x16x32_bf16 v[96:99], v[48:51], a[32:35], v[96:99]\n\t"
      "buffer_load_dwordx4 v12, s[52:55], s39 offen offset:2048 sc1 lds\n\t"
      "v_mfma_f32_16x16x32_bf16 v[124:127], a[240:243], a[88:91], 0\n\t"
      "ds_write_b64 v17, v[212:213]\n\t"
      "v_mfma_f32_16x16x32_bf16 v[128:131], a[240:243], a[92:95], 0\n\t"
      "v_cvt_pk_bf16_f32 v232, v232, v233\n\t"
      "v_cvt_pk_bf16_f32 v225, v226, v227\n\t"
      "v_mfma_f32_16x16x32_bf16 v[132:135], a[244:247], a[92:95], 0\n\t"
      "s_waitcnt lgkmcnt(5)\n\t"
      "v_cvt_pk_bf16_f32 v229, v230, v231\n\t"
      "v_cvt_pk_bf16_f32 v233, v234, v235\n\t"
      "v_mfma_f32_16x16x32_bf16 v[88:91], v[52:55], a[36:39], v[88:91]\n\t"
      "buffer_store_dwordx2 v[248:249], v250, s[52:55], s65 offen sc1\n\t"
      "v_accvgpr_write_b32 a154, v174\n\t"
      "v_mfma_f32_16x16x32_bf16 v[92:95], v[52:55], a[40:43], v[92:95]\n\t"
      "ds_write_b64 v17, v[216:217] offset:8\n\t"
      "v_mfma_f32_16x16x32_bf16 v[96:99], v[52:55], a[44:47], v[96:99]\n\t"
      "v_accvgpr_write_b32 a155, v175\n\t"
      "v_accvgpr_write_b32 a170, v178\n\t"
      "v_mfma_f32_16x16x32_bf16 v[88:91], v[56:59], a[48:51], v[88:91]\n\t"
      "v_accvgpr_write_b32 a171, v179\n\t"
      "v_accvgpr_write_b32 a186, v182\n\t"
      "v_mfma_f32_16x16x32_bf16 v[92:95], v[56:59], a[52:55], v[92:95]\n\t"
      "ds_write_b64 v24, v[220:221]\n\t"
      "v_mfma_f32_16x16x32_bf16 v[96:99], v[56:59], a[56:59], v[96:99]\n\t"
      "v_accvgpr_write_b32 a187, v183\n\t"
      "v_bfe_u32 v4, v237, 0, 2\n\t"
      "v_mfma_f32_16x16x32_bf16 v[88:91], v[60:63], a[60:63], v[88:91]\n\t"
      "v_mad_u32_u24 v4, v4, s76, v238\n\t"
      "v_mov_b32_e32 v188, 0\n\t"
      "v_mfma_f32_16x16x32_bf16 v[92:95], v[60:63], a[64:67], v[92:95]\n\t"
      "ds_write_b64 v17, v[224:225] offset:12288\n\t"
      "v_mfma_f32_16x16x32_bf16 v[96:99], v[60:63], a[68:71], v[96:99]\n\t"
      "s_waitcnt lgkmcnt(3)\n\t"
      "s_barrier\n\t"
      "s_nop 0\n\t"
      "v_mfma_f32_16x16x32_bf16 v[100:103], v[64:67], a[0:3], v[124:127]\n\t"
      "buffer_load_dwordx4 a[192:195], v12, s[52:55], s69 offen sc1\n\t"
      "v_cvt_pk_bf16_f32 v172, v88, v89\n\t"
      "v_mfma_f32_16x16x32_bf16 v[104:107], v[64:67], a[4:7], v[128:131]\n\t"
      "ds_read_b128 v[40:43], v10 offset:24576\n\t"
      "v_cvt_pk_bf16_f32 v173, v90, v91\n\t"
      "v_mfma_f32_16x16x32_bf16 v[108:111], v[64:67], a[8:11], v[132:135]\n\t"
      "v_cvt_pk_bf16_f32 v176, v92, v93\n\t"
      "v_cvt_pk_bf16_f32 v177, v94, v95\n\t"
      "v_mfma_f32_16x16x32_bf16 v[100:103], v[68:71], a[12:15], v[100:103]\n\t"
      "ds_read_b128 v[44:47], v11 offset:24576\n\t"
      "v_cvt_pk_bf16_f32 v180, v96, v97\n\t"
      "v_mfma_f32_16x16x32_bf16 v[200:203], v[172:175], a[80:83], 0\n\t"
      "v_cvt_pk_bf16_f32 v181, v98, v99\n\t"
      "v_accvgpr_write_b32 a108, v172\n\t"
      "v_mfma_f32_16x16x32_bf16 v[204:207], v[176:179], a[80:83], 0\n\t"
      "buffer_load_dwordx4 a[200:203], v12, s[52:55], s69 offen offset:1024 sc1\n\t"
      "v_accvgpr_write_b32 a109, v173\n\t"
      "v_mfma_f32_16x16x32_bf16 v[208:211], v[180:183], a[80:83], 0\n\t"
      "ds_read_b128 v[48:51], v10 offset:26624\n\t"
      "v_accvgpr_write_b32 a124, v176\n\t"
      "v_mfma_f32_16x16x32_bf16 v[104:107], v[68:71], a[16:19], v[104:107]\n\t"
      "ds_write_b64 v17, v[228:229] offset:12296\n\t"
      "v_mfma_f32_16x16x32_bf16 v[108:111], v[68:71], a[20:23], v[108:111]\n\t"
      "v_cvt_pk_bf16_f32 v200, v200, v201\n\t"
      "v_cvt_pk_bf16_f32 v201, v202, v203\n\t"
      "v_mfma_f32_16x16x32_bf16 v[100:103], v[72:75], a[24:27], v[100:103]\n\t"
      "buffer_load_dwordx4 a[208:211], v12, s[52:55], s69 offen offset:2048 sc1\n\t"
      "s_add_u32 s69, s69, 0xc00\n\t"
      "v_mfma_f32_16x16x32_bf16 v[104:107], v[72:75], a[28:31], v[104:107]\n\t"
      "ds_read_b128 v[52:55], v11 offset:26624\n\t"
      "v_cvt_pk_bf16_f32 v204, v204, v205\n\t"
      "v_mfma_f32_16x16x32_bf16 v[108:111], v[72:75], a[32:35], v[108:111]\n\t"
      "ds_write_b64 v24, v[232:233] offset:12288\n\t"
      "v_mfma_f32_16x16x32_bf16 v[100:103], v[76:79], a[36:39], v[100:103]\n\t"
      "v_accvgpr_write_b32 a125, v177\n\t"
      "v_accvgpr_write_b32 a140, v180\n\t"
      "v_mfma_f32_16x16x32_bf16 v[104:107], v[76:79], a[40:43], v[104:107]\n\t"
      "buffer_load_dwordx4 a[196:199], v12, s[52:55], s69 offen sc1\n\t"
      "v_accvgpr_write_b32 a141, v181\n\t"
      "v_mfma_f32_16x16x32_bf16 v[212:215], v[172:175], a[72:75], 0\n\t"
      "ds_read_b128 v[56:59], v10 offset:28672\n\t"
      "v_cvt_pk_bf16_f32 v205, v206, v207\n\t"
      "v_mfma_f32_16x16x32_bf16 v[216:219], v[176:179], a[72:75], 0\n\t"
      "ds_write_b64 v18, v[200:201]\n\t"
      "v_mfma_f32_16x16x32_bf16 v[220:223], v[180:183], a[72:75], 0\n\t"
      "v_cvt_pk_bf16_f32 v208, v208, v209\n\t"
      "v_cvt_pk_bf16_f32 v209, v210, v211\n\t"
      "v_mfma_f32_16x16x32_bf16 v[108:111], v[76:79], a[44:47], v[108:111]\n\t"
      "buffer_load_dwordx4 a[204:207], v12, s[52:55], s69 offen offset:1024 sc1\n\t"
      "v_cvt_pk_bf16_f32 v212, v212, v213\n\t"
      "v_mfma_f32_16x16x32_bf16 v[100:103], v[80:83], a[48:51], v[100:103]\n\t"
      "ds_read_b128 v[60:63], v11 offset:28672\n\t"
      "v_cvt_pk_bf16_f32 v216, v216, v217\n\t"
      "v_mfma_f32_16x16x32_bf16 v[104:107], v[80:83], a[52:55], v[104:107]\n\t"
      "ds_write_b64 v18, v[204:205] offset:8\n\t"
      "v_mfma_f32_16x16x32_bf16 v[108:111], v[80:83], a[56:59], v[108:111]\n\t"
      "v_cvt_pk_bf16_f32 v220, v220, v221\n\t"
      "v_cvt_pk_bf16_f32 v213, v214, v215\n\t"
      "v_mfma_f32_16x16x32_bf16 v[100:103], v[84:87], a[60:63], v[100:103]\n\t"
      "buffer_load_dwordx4 a[212:215], v12, s[52:55], s69 offen offset:2048 sc1\n\t"
      "s_add_u32 s69, s69, 0xc00\n\t"
      "v_mfma_f32_16x16x32_bf16 v[104:107], v[84:87], a[64:67], v[104:107]\n\t"
      "ds_read_b128 v[64:67], v10 offset:30720\n\t"
      "v_cvt_pk_bf16_f32 v217, v218, v219\n\t"
      "v_mfma_f32_16x16x32_bf16 v[108:111], v[84:87], a[68:71], v[108:111]\n\t"
      "s_waitcnt lgkmcnt(5)\n\t"
      "s_nop 0\n\t"
      "v_cvt_pk_bf16_f32 v150, v100, v101\n\t"
      "v_cvt_pk_bf16_f32 v151, v102, v103\n\t"
      "v_mfma_f32_16x16x32_bf16 v[88:91], v[40:43], a[0:3], v[124:127]\n\t"
      "ds_read_b128 v[68:71], v11 offset:30720\n\t"
      "v_cvt_pk_bf16_f32 v154, v104, v105\n\t"
      "v_mfma_f32_16x16x32_bf16 v[92:95], v[40:43], a[4:7], v[128:131]\n\t"
      "buffer_load_dwordx4 a[216:219], v12, s[52:55], s69 offen sc1\n\t"
      "v_cvt_pk_bf16_f32 v155, v106, v107\n\t"
      "v_mfma_f32_16x16x32_bf16 v[96:99], v[40:43], a[8:11], v[132:135]\n\t"
      "ds_read_b128 v[72:75], v10 offset:32768\n\t"
      "v_cvt_pk_bf16_f32 v158, v108, v109\n\t"
      "v_mfma_f32_16x16x32_bf16 v[88:91], v[44:47], a[12:15], v[88:91]\n\t"
      "v_cvt_pk_bf16_f32 v159, v110, v111\n\t"
      "v_cvt_pk_bf16_f32 v221, v222, v223\n\t"
      "v_mfma_f32_16x16x32_bf16 v[92:95], v[44:47], a[16:19], v[92:95]\n\t"
      "ds_read_b128 v[76:79], v11 offset:32768\n\t"
      "v_accvgpr_write_b32 a156, v150\n\t"
      "v_mfma_f32_16x16x32_bf16 v[96:99], v[44:47], a[20:23], v[96:99]\n\t"
      "buffer_load_dwordx4 a[224:227], v12, s[52:55], s69 offen offset:1024 sc1\n\t"
      "v_accvgpr_write_b32 a157, v151\n\t"
      "v_mfma_f32_16x16x32_bf16 v[88:91], v[48:51], a[24:27], v[88:91]\n\t"
      "ds_read_b128 v[80:83], v10 offset:34816\n\t"
      "v_accvgpr_write_b32 a172, v154\n\t"
      "v_mfma_f32_16x16x32_bf16 v[92:95], v[48:51], a[28:31], v[92:95]\n\t"
      "ds_write_b64 v25, v[208:209]\n\t"
      "v_mfma_f32_16x16x32_bf16 v[96:99], v[48:51], a[32:35], v[96:99]\n\t"
      "v_accvgpr_write_b32 a173, v155\n\t"
      "v_accvgpr_write_b32 a188, v158\n\t"
      "v_mfma_f32_16x16x32_bf16 v[112:115], a[248:251], a[88:91], 0\n\t"
      "buffer_load_dwordx4 a[232:235], v12, s[52:55], s69 offen offset:2048 sc1\n\t"
      "s_add_u32 s69, s69, 0xc00\n\t"
      "v_mfma_f32_16x16x32_bf16 v[116:119], a[248:251], a[92:95], 0\n\t"
      "ds_read_b128 v[84:87], v11 offset:34816\n\t"
      "v_accvgpr_write_b32 a189, v159\n\t"
      "v_mfma_f32_16x16x32_bf16 v[120:123], a[252:255], a[92:95], 0\n\t"
      "ds_write_b64 v18, v[212:213] offset:12288\n\t"
      "v_mfma_f32_16x16x32_bf16 v[88:91], v[52:55], a[36:39], v[88:91]\n\t"
      "v_mov_b32_e32 v189, 0\n\t"
      "v_mov_b32_e32 v190, 0\n\t"
      "v_mfma_f32_16x16x32_bf16 v[92:95], v[52:55], a[40:43], v[92:95]\n\t"
      "buffer_load_dwordx4 a[220:223], v12, s[52:55], s69 offen sc1\n\t"
      "v_mov_b32_e32 v191, 0\n\t"
      "v_mfma_f32_16x16x32_bf16 v[96:99], v[52:55], a[44:47], v[96:99]\n\t"
      "s_waitcnt lgkmcnt(5)\n\t"
      "v_mov_b32_e32 v192, 0\n\t"
      "v_mov_b32_e32 v193, 0\n\t"
      "v_mfma_f32_16x16x32_bf16 v[88:91], v[56:59], a[48:51], v[88:91]\n\t"
      "ds_write_b64 v18, v[216:217] offset:12296\n\t"
      "v_mfma_f32_16x16x32_bf16 v[92:95], v[56:59], a[52:55], v[92:95]\n\t"
      "v_mov_b32_e32 v194, 0\n\t"
      "v_mov_b32_e32 v195, 0\n\t"
      "v_mfma_f32_16x16x32_bf16 v[96:99], v[56:59], a[56:59], v[96:99]\n\t"
      "buffer_load_dwordx4 a[228:231], v12, s[52:55], s69 offen offset:1024 sc1\n\t"
      "v_mov_b32_e32 v196, 0\n\t"
      "v_mfma_f32_16x16x32_bf16 v[88:91], v[60:63], a[60:63], v[88:91]\n\t"
      "ds_write_b64 v25, v[220:221] offset:12288\n\t"
      "v_mfma_f32_16x16x32_bf16 v[92:95], v[60:63], a[64:67], v[92:95]\n\t"
      "v_mov_b32_e32 v197, 0\n\t"
      "v_mov_b32_e32 v198, 0\n\t"
      "v_mfma_f32_16x16x32_bf16 v[96:99], v[60:63], a[68:71], v[96:99]\n\t"
      "v_mov_b32_e32 v199, 0\n\t"
      "v_and_b32_e32 v144, 7, v37\n\t"
      "v_mfma_f32_16x16x32_bf16 v[100:103], v[64:67], a[0:3], v[112:115]\n\t"
      "buffer_load_dwordx4 a[236:239], v12, s[52:55], s69 offen offset:2048 sc1\n\t"
      "s_add_u32 s69, s69, 0xc00\n\t"
      "v_mfma_f32_16x16x32_bf16 v[104:107], v[64:67], a[4:7], v[116:119]\n\t"
      "v_cvt_pk_bf16_f32 v148, v88, v89\n\t"
      "v_cvt_pk_bf16_f32 v149, v90, v91\n\t"
      "v_mfma_f32_16x16x32_bf16 v[108:111], v[64:67], a[8:11], v[120:123]\n\t"
      "v_cvt_pk_bf16_f32 v152, v92, v93\n\t"
      "v_cvt_pk_bf16_f32 v153, v94, v95\n\t"
      "v_mfma_f32_16x16x32_bf16 v[100:103], v[68:71], a[12:15], v[100:103]\n\t"
      "v_cvt_pk_bf16_f32 v156, v96, v97\n\t"
      "v_cvt_pk_bf16_f32 v157, v98, v99\n\t"
      "v_mfma_f32_16x16x32_bf16 v[224:227], v[148:151], a[80:83], 0\n\t"
      "buffer_load_dwordx4 a[0:3], v12, s[52:55], s69 offen sc1\n\t"
      "v_accvgpr_write_b32 a110, v148\n\t"
      "v_mfma_f32_16x16x32_bf16 v[228:231], v[152:155], a[80:83], 0\n\t"
      "v_accvgpr_write_b32 a111, v149\n\t"
      "v_accvgpr_write_b32 a126, v152\n\t"
      "v_mfma_f32_16x16x32_bf16 v[232:235], v[156:159], a[80:83], 0\n\t"
      "v_accvgpr_write_b32 a127, v153\n\t"
      "v_accvgpr_write_b32 a142, v156\n\t"
      "v_mfma_f32_16x16x32_bf16 v[104:107], v[68:71], a[16:19], v[104:107]\n\t"
      "v_cvt_pk_bf16_f32 v224, v224, v225\n\t"
      "v_cvt_pk_bf16_f32 v225, v226, v227\n\t"
      "v_mfma_f32_16x16x32_bf16 v[108:111], v[68:71], a[20:23], v[108:111]\n\t"
      "buffer_load_dwordx4 a[8:11], v12, s[52:55], s69 offen offset:1024 sc1\n\t"
      "v_cvt_pk_bf16_f32 v228, v228, v229\n\t"
      "v_mfma_f32_16x16x32_bf16 v[100:103], v[72:75], a[24:27], v[100:103]\n\t"
      "v_cvt_pk_bf16_f32 v232, v232, v233\n\t"
      "v_cvt_pk_bf16_f32 v229, v230, v231\n\t"
      "v_mfma_f32_16x16x32_bf16 v[104:107], v[72:75], a[28:31], v[104:107]\n\t"
      "v_cvt_pk_bf16_f32 v233, v234, v235\n\t"
      "v_accvgpr_write_b32 a143, v157\n\t"
      "v_mfma_f32_16x16x32_bf16 v[108:111], v[72:75], a[32:35], v[108:111]\n\t"
      "s_waitcnt lgkmcnt(0)\n\t"
      "v_mul_u32_u24_e32 v145, 0x180, v37\n\t"
      "s_mul_i32 s60, s56, 0x60\n\t"
      "v_mfma_f32_16x16x32_bf16 v[100:103], v[76:79], a[36:39], v[100:103]\n\t"
      "ds_write_b64 v19, v[224:225]\n\t"
      "v_mfma_f32_16x16x32_bf16 v[104:107], v[76:79], a[40:43], v[104:107]\n\t"
      "buffer_load_dwordx4 a[16:19], v12, s[52:55], s69 offen offset:2048 sc1\n\t"
      "s_add_u32 s69, s69, 0xc00\n\t"
      "v_mfma_f32_16x16x32_bf16 v[200:203], v[148:151], a[72:75], 0\n\t"
      "s_add_u32 s60, s60, 0x26400\n\t"
      "v_cmp_gt_u32_e32 vcc, 2, v38\n\t"
      "v_and_b32_e32 v147, 1, v38\n\t"
      "v_mfma_f32_16x16x32_bf16 v[204:207], v[152:155], a[72:75], 0\n\t"
      "ds_write_b64 v19, v[228:229] offset:8\n\t"
      "v_mfma_f32_16x16x32_bf16 v[208:211], v[156:159], a[72:75], 0\n\t"
      "v_mfma_f32_16x16x32_bf16 v[108:111], v[76:79], a[44:47], v[108:111]\n\t"
      "s_nop 0\n\t"
      "v_cvt_pk_bf16_f32 v200, v200, v201\n\t"
      "v_cvt_pk_bf16_f32 v201, v202, v203\n\t"
      "v_mfma_f32_16x16x32_bf16 v[100:103], v[80:83], a[48:51], v[100:103]\n\t"
      "ds_write_b64 v27, v[200:201]\n\t"
      "v_mfma_f32_16x16x32_bf16 v[104:107], v[80:83], a[52:55], v[104:107]\n\t"
      "v_cvt_pk_bf16_f32 v204, v204, v205\n\t"
      "v_cvt_pk_bf16_f32 v205, v206, v207\n\t"
      "v_mfma_f32_16x16x32_bf16 v[108:111], v[80:83], a[56:59], v[108:111]\n\t"
      "ds_write_b64 v27, v[204:205] offset:8\n\t"
      "v_mfma_f32_16x16x32_bf16 v[100:103], v[84:87], a[60:63], v[100:103]\n\t"
      "v_cvt_pk_bf16_f32 v208, v208, v209\n\t"
      "v_cvt_pk_bf16_f32 v209, v210, v211\n\t"
      "v_mfma_f32_16x16x32_bf16 v[104:107], v[84:87], a[64:67], v[104:107]\n\t"
      "v_mov_b32_e32 v200, 0\n\t"
      "v_mov_b32_e32 v201, 0\n\t"
      "v_mfma_f32_16x16x32_bf16 v[108:111], v[84:87], a[68:71], v[108:111]\n\t"
      "v_mov_b32_e32 v202, 0\n\t"
      "v_mov_b32_e32 v203, 0\n\t"
      "v_add_u32_e32 v204, s59, v12\n\t"
      "ds_write_b64 v26, v[232:233]\n\t"
      "ds_write_b64 v30, v[208:209]\n\t"
      "ds_write_b128 v204, v[200:203]\n\t"
      "buffer_load_dwordx4 a[4:7], v12, s[52:55], s69 offen sc1\n\t"
      "buffer_load_dwordx4 a[12:15], v12, s[52:55], s69 offen offset:1024 sc1\n\t"
      "s_mul_i32 s59, s56, 6\n\t"
      "v_add_u32_e32 v146, s59, v38\n\t"
      "v_xor_b32_e32 v146, v146, v144\n\t"
      "v_lshl_add_u32 v239, v146, 4, v145\n\t"
      "v_lshrrev_b32_e32 v146, 1, v38\n\t"
      "v_add3_u32 v146, v146, s59, 4\n\t"
      "v_xor_b32_e32 v146, v146, v144\n\t"
      "v_lshrrev_b32_e32 v144, 2, v37\n\t"
      "v_lshl_add_u32 v146, v146, 4, v145\n\t"
      "s_waitcnt lgkmcnt(0)\n\t"
      "v_lshlrev_b32_e32 v144, 3, v144\n\t"
      "v_mul_u32_u24_e32 v145, 0x300, v38\n\t"
      "s_barrier\n\t"
      "ds_read_b128 a[240:243], v33\n\t"
      "ds_read_b128 a[244:247], v34\n\t"
      "ds_read_b128 a[248:251], v35\n\t"
      "ds_read_b128 a[252:255], v33 offset:1024\n\t"
      "buffer_load_dwordx4 a[20:23], v12, s[52:55], s69 offen offset:2048 sc1\n\t"
      "s_add_u32 s69, s69, 0xc00\n\t"
      "buffer_load_dwordx4 a[24:27], v12, s[52:55], s69 offen sc1\n\t"
      "ds_read_b128 a[88:91], v34 offset:1024\n\t"
      "ds_read_b128 a[92:95], v35 offset:1024\n\t"
      "ds_read2_b32 v[0:1], v4 offset0:0 offset1:2\n\t"
      "ds_read2_b32 v[2:3], v4 offset0:4 offset1:6\n\t"
      "ds_read2_b32 v[8:9], v4 offset0:32 offset1:34\n\t"
      "ds_read2_b32 v[10:11], v4 offset0:36 offset1:38\n\t"
      "v_lshl_add_u32 v144, v37, 1, v144\n\t"
      "v_add_u32_e32 v145, s60, v145\n\t"
      "v_add_u32_e32 v144, v144, v145\n\t"
      "v_lshl_add_u32 v145, v37, 1, v145\n\t"
      "v_cndmask_b32_e32 v242, v39, v144, vcc\n\t"
      "v_cndmask_b32_e32 v246, v39, v145, vcc\n\t"
      "s_waitcnt vmcnt(16) lgkmcnt(9)\n\t"
      "v_cmp_gt_u32_e32 vcc, 0x180, v12\n\t"
      "v_lshl_add_u32 v243, v147, 3, v146\n\t"
      "v_mfma_f32_16x16x32_bf16 v[40:43], a[192:195], a[240:243], 0\n\t"
      "s_mul_i32 s60, s56, 0x180\n\ts_add_u32 s60, s60, 0x26400\n\ts_lshr_b32 s61, s56, 1\n\ts_add_u32 s61, s61, 12\n\ts_mul_i32 s61, s61, 0x15000\n\ts_and_b32 s62, s56, 1\n\ts_add_u32 s62, s62, 12\n\ts_mul_i32 s62, s62, 0x180\n\ts_add_u32 s61, s61, s62\n\t"
      "v_mfma_f32_16x16x32_bf16 v[44:47], a[200:203], a[240:243], 0\n\t"
      "buffer_load_dwordx4 a[32:35], v12, s[52:55], s69 offen offset:1024 sc1\n\t"
      "v_add_u32_e32 v144, s61, v12\n\t"
      "v_mfma_f32_16x16x32_bf16 v[48:51], a[208:211], a[240:243], 0\n\t"
      "s_waitcnt lgkmcnt(6)\n\t"
      "ds_read_b128 a[240:243], v33 offset:2048\n\t"
      "v_mfma_f32_16x16x32_bf16 v[52:55], a[192:195], a[244:247], 0\n\t"
      "v_add_u32_e32 v6, s60, v12\n\t"
      "v_cndmask_b32_e32 v7, v39, v144, vcc\n\t"
      "v_mfma_f32_16x16x32_bf16 v[56:59], a[200:203], a[244:247], 0\n\t"
      "s_add_u32 s59, s56, 0\n\ts_mul_i32 s60, s59, 43\n\ts_lshr_b32 s60, s60, 8\n\ts_mul_i32 s61, s60, 6\n\ts_sub_u32 s61, s59, s61\n\ts_lshl_b32 s61, s61, 10\n\ts_mul_i32 s62, s60, 0xc000\n\ts_add_u32 s62, s62, s61\n\ts_lshl_b32 s60, s60, 4\n\t"
      "v_mfma_f32_16x16x32_bf16 v[60:63], a[208:211], a[244:247], 0\n\t"
      "buffer_load_dwordx4 a[40:43], v12, s[52:55], s69 offen offset:2048 sc1\n\t"
      "s_add_u32 s69, s69, 0xc00\n\t"
      "v_mfma_f32_16x16x32_bf16 v[64:67], a[192:195], a[248:251], 0\n\t"
      "ds_read_b128 a[244:247], v34 offset:2048\n\t"
      "v_add_u32_e32 v144, s61, v12\n\t"
      "v_mfma_f32_16x16x32_bf16 v[68:71], a[200:203], a[248:251], 0\n\t"
      "v_lshrrev_b32_e32 v144, 4, v144\n\t"
      "v_add_u32_e32 v247, s62, v12\n\t"
      "v_mfma_f32_16x16x32_bf16 v[72:75], a[208:211], a[248:251], 0\n\t"
      "s_waitcnt vmcnt(15)\n\t"
      "ds_read_b128 a[248:251], v35 offset:2048\n\t"
      "v_mfma_f32_16x16x32_bf16 v[40:43], a[196:199], a[252:255], v[40:43]\n\t"
      "buffer_load_dwordx4 a[28:31], v12, s[52:55], s69 offen sc1\n\t"
      "v_mul_u32_u24_e32 v145, 0xaab, v144\n\t"
      "v_mfma_f32_16x16x32_bf16 v[44:47], a[204:207], a[252:255], v[44:47]\n\t"
      "v_lshrrev_b32_e32 v145, 16, v145\n\t"
      "v_cvt_pk_bf16_f32 v100, v100, v101\n\t"
      "v_mfma_f32_16x16x32_bf16 v[48:51], a[212:215], a[252:255], v[48:51]\n\t"
      "s_waitcnt lgkmcnt(3)\n\t"
      "ds_read_b128 a[252:255], v33 offset:3072\n\t"
      "v_mfma_f32_16x16x32_bf16 v[52:55], a[196:199], a[88:91], v[52:55]\n\t"
      "v_mul_u32_u24_e32 v146, 24, v145\n\t"
      "v_cvt_pk_bf16_f32 v104, v104, v105\n\t"
      "v_mfma_f32_16x16x32_bf16 v[56:59], a[204:207], a[88:91], v[56:59]\n\t"
      "buffer_load_dwordx4 a[36:39], v12, s[52:55], s69 offen offset:1024 sc1\n\t"
      "v_sub_u32_e32 v144, v144, v146\n\t"
      "v_mfma_f32_16x16x32_bf16 v[60:63], a[212:215], a[88:91], v[60:63]\n\t"
      "ds_read_b128 a[88:91], v34 offset:3072\n\t"
      "v_and_b32_e32 v146, 7, v145\n\t"
      "v_mfma_f32_16x16x32_bf16 v[64:67], a[196:199], a[92:95], v[64:67]\n\t"
      "v_add_u32_e32 v145, s60, v145\n\t"
      "v_xor_b32_e32 v144, v144, v146\n\t"
      "v_mfma_f32_16x16x32_bf16 v[68:71], a[204:207], a[92:95], v[68:71]\n\t"
      "v_mul_u32_u24_e32 v146, 37, v145\n\t"
      "v_cmp_gt_u32_e32 vcc, 48, v145\n\t"
      "v_mfma_f32_16x16x32_bf16 v[72:75], a[212:215], a[92:95], v[72:75]\n\t"
      "buffer_load_dwordx4 a[44:47], v12, s[52:55], s69 offen offset:2048 sc1\n\t"
      "s_add_u32 s69, s69, 0xc00\n\t"
      "v_smfmac_f32_16x16x64_bf16 v[188:191], v[0:3], a[192:199], v236\n\t"
      "ds_read_b128 a[92:95], v35 offset:3072\n\t"
      "v_lshrrev_b32_e32 v146, 8, v146\n\t"
      "v_smfmac_f32_16x16x64_bf16 v[192:195], v[0:3], a[200:207], v236\n\t"
      "v_mul_u32_u24_e32 v147, 7, v146\n\t"
      "v_mul_u32_u24_e32 v146, 0x2a000, v146\n\t"
      "v_smfmac_f32_16x16x64_bf16 v[196:199], v[0:3], a[208:215], v236\n\t"
      "ds_read2_b32 v[0:1], v4 offset0:64 offset1:66\n\t"
      "v_sub_u32_e32 v147, v145, v147\n\t"
      "v_mfma_f32_16x16x32_bf16 v[212:215], v[160:163], a[84:87], 0\n\t"
      "buffer_load_dwordx4 a[48:51], v12, s[52:55], s69 offen sc1\n\t"
      "v_mul_u32_u24_e32 v147, 0x300, v147\n\t"
      "v_mfma_f32_16x16x32_bf16 v[216:219], v[164:167], a[84:87], 0\n\t"
      "ds_read2_b32 v[2:3], v4 offset0:68 offset1:70\n\t"
      "v_lshl_add_u32 v144, v144, 4, v146\n\t"
      "v_mfma_f32_16x16x32_bf16 v[220:223], v[168:171], a[84:87], 0\n\t"
      "s_waitcnt vmcnt(16) lgkmcnt(4)\n\t"
      "v_add_u32_e32 v144, v144, v147\n\t"
      "v_cvt_pk_bf16_f32 v108, v108, v109\n\t"
      "v_mfma_f32_16x16x32_bf16 v[40:43], a[216:219], a[240:243], v[40:43]\n\t"
      "v_cndmask_b32_e32 v252, v39, v144, vcc\n\t"
      "v_cvt_pk_bf16_f32 v101, v102, v103\n\t"
      "v_mfma_f32_16x16x32_bf16 v[44:47], a[224:227], a[240:243], v[44:47]\n\t"
      "buffer_load_dwordx4 a[56:59], v12, s[52:55], s69 offen offset:1024 sc1\n\t"
      "v_cvt_pk_bf16_f32 v105, v106, v107\n\t"
      "v_mfma_f32_16x16x32_bf16 v[48:51], a[232:235], a[240:243], v[48:51]\n\t"
      "ds_read_b128 a[240:243], v33 offset:4096\n\t"
      "v_cvt_pk_bf16_f32 v109, v110, v111\n\t"
      "v_mfma_f32_16x16x32_bf16 v[52:55], a[216:219], a[244:247], v[52:55]\n\t"
      "s_add_u32 s59, s56, 4\n\ts_mul_i32 s60, s59, 43\n\ts_lshr_b32 s60, s60, 8\n\ts_mul_i32 s61, s60, 6\n\ts_sub_u32 s61, s59, s61\n\ts_lshl_b32 s61, s61, 10\n\ts_mul_i32 s62, s60, 0xc000\n\ts_add_u32 s62, s62, s61\n\ts_lshl_b32 s60, s60, 4\n\t"
      "v_mfma_f32_16x16x32_bf16 v[56:59], a[224:227], a[244:247], v[56:59]\n\t"
      "buffer_load_dwordx4 a[64:67], v12, s[52:55], s69 offen offset:2048 sc1\n\t"
      "s_add_u32 s69, s69, 0xc00\n\t"
      "v_mfma_f32_16x16x32_bf16 v[60:63], a[232:235], a[244:247], v[60:63]\n\t"
      "ds_read_b128 a[244:247], v34 offset:4096\n\t"
      "v_add_u32_e32 v144, s61, v12\n\t"
      "v_mfma_f32_16x16x32_bf16 v[64:67], a[216:219], a[248:251], v[64:67]\n\t"
      "v_lshrrev_b32_e32 v144, 4, v144\n\t"
      "v_add_u32_e32 v248, s62, v12\n\t"
      "v_mfma_f32_16x16x32_bf16 v[68:71], a[224:227], a[248:251], v[68:71]\n\t"
      "v_mul_u32_u24_e32 v145, 0xaab, v144\n\t"
      "v_accvgpr_write_b32 a158, v100\n\t"
      "v_mfma_f32_16x16x32_bf16 v[72:75], a[232:235], a[248:251], v[72:75]\n\t"
      "buffer_load_dwordx4 a[52:55], v12, s[52:55], s69 offen sc1\n\t"
      "v_lshrrev_b32_e32 v145, 16, v145\n\t"
      "v_mfma_f32_16x16x32_bf16 v[224:227], v[160:163], a[76:79], 0\n\t"
      "ds_read_b128 a[248:251], v35 offset:4096\n\t"
      "v_mul_u32_u24_e32 v146, 24, v145\n\t"
      "v_mfma_f32_16x16x32_bf16 v[228:231], v[164:167], a[76:79], 0\n\t"
      "v_sub_u32_e32 v144, v144, v146\n\t"
      "v_and_b32_e32 v146, 7, v145\n\t"
      "v_mfma_f32_16x16x32_bf16 v[232:235], v[168:171], a[76:79], 0\n\t"
      "s_waitcnt vmcnt(16)\n\t"
      "v_add_u32_e32 v145, s60, v145\n\t"
      "v_xor_b32_e32 v144, v144, v146\n\t"
      "v_mfma_f32_16x16x32_bf16 v[40:43], a[220:223], a[252:255], v[40:43]\n\t"
      "buffer_load_dwordx4 a[60:63], v12, s[52:55], s69 offen offset:1024 sc1\n\t"
      "v_mul_u32_u24_e32 v146, 37, v145\n\t"
      "v_mfma_f32_16x16x32_bf16 v[44:47], a[228:231], a[252:255], v[44:47]\n\t"
      "v_lshrrev_b32_e32 v146, 8, v146\n\t"
      "v_cmp_gt_u32_e32 vcc, 48, v145\n\t"
      "v_mfma_f32_16x16x32_bf16 v[48:51], a[236:239], a[252:255], v[48:51]\n\t"
      "s_waitcnt lgkmcnt(2)\n\t"
      "ds_read_b128 a[252:255], v33 offset:5120\n\t"
      "v_mfma_f32_16x16x32_bf16 v[52:55], a[220:223], a[88:91], v[52:55]\n\t"
      "v_mul_u32_u24_e32 v147, 7, v146\n\t"
      "v_mul_u32_u24_e32 v146, 0x2a000, v146\n\t"
      "v_mfma_f32_16x16x32_bf16 v[56:59], a[228:231], a[88:91], v[56:59]\n\t"
      "v_sub_u32_e32 v147, v145, v147\n\t"
      "v_lshl_add_u32 v144, v144, 4, v146\n\t"
      "v_mfma_f32_16x16x32_bf16 v[60:63], a[236:239], a[88:91], v[60:63]\n\t"
      "buffer_load_dwordx4 a[68:71], v12, s[52:55], s69 offen offset:2048 sc1\n\t"
      "s_add_u32 s69, s69, 0xc00\n\t"
      "v_mfma_f32_16x16x32_bf16 v[64:67], a[220:223], a[92:95], v[64:67]\n\t"
      "ds_read_b128 a[88:91], v34 offset:5120\n\t"
      "v_mul_u32_u24_e32 v147, 0x300, v147\n\t"
      "v_mfma_f32_16x16x32_bf16 v[68:71], a[228:231], a[92:95], v[68:71]\n\t"
      "v_add_u32_e32 v144, v144, v147\n\t"
      "v_accvgpr_write_b32 a159, v101\n\t"
      "v_mfma_f32_16x16x32_bf16 v[72:75], a[236:239], a[92:95], v[72:75]\n\t"
      "ds_read_b128 a[92:95], v35 offset:5120\n\t"
      "v_cndmask_b32_e32 v253, v39, v144, vcc\n\t"
      "v_smfmac_f32_16x16x64_bf16 v[188:191], v[8:11], a[216:223], v236\n\t"
      "buffer_load_dwordx4 a[192:195], v12, s[52:55], s69 offen sc1\n\t"
      "v_accvgpr_write_b32 a174, v104\n\t"
      "v_smfmac_f32_16x16x64_bf16 v[192:195], v[8:11], a[224:231], v236\n\t"
      "s_add_u32 s59, s56, 8\n\ts_mul_i32 s60, s59, 43\n\ts_lshr_b32 s60, s60, 8\n\ts_mul_i32 s61, s60, 6\n\ts_sub_u32 s61, s59, s61\n\ts_lshl_b32 s61, s61, 10\n\ts_mul_i32 s62, s60, 0xc000\n\ts_add_u32 s62, s62, s61\n\ts_lshl_b32 s60, s60, 4\n\t"
      "v_smfmac_f32_16x16x64_bf16 v[196:199], v[8:11], a[232:239], v236\n\t"
      "s_waitcnt vmcnt(16)\n\t"
      "v_add_u32_e32 v144, s61, v12\n\t"
      "v_accvgpr_write_b32 a175, v105\n\t"
      "v_mfma_f32_16x16x32_bf16 v[40:43], a[0:3], a[240:243], v[40:43]\n\t"
      "buffer_load_dwordx4 a[200:203], v12, s[52:55], s69 offen offset:1024 sc1\n\t"
      "v_lshrrev_b32_e32 v144, 4, v144\n\t"
      "v_mfma_f32_16x16x32_bf16 v[44:47], a[8:11], a[240:243], v[44:47]\n\t"
      "v_mul_u32_u24_e32 v145, 0xaab, v144\n\t"
      "v_accvgpr_write_b32 a190, v108\n\t"
      "v_mfma_f32_16x16x32_bf16 v[48:51], a[16:19], a[240:243], v[48:51]\n\t"
      "s_waitcnt lgkmcnt(2)\n\t"
      "ds_read_b128 a[240:243], v33 offset:6144\n\t"
      "v_mfma_f32_16x16x32_bf16 v[52:55], a[0:3], a[244:247], v[52:55]\n\t"
      "v_lshrrev_b32_e32 v145, 16, v145\n\t"
      "v_accvgpr_write_b32 a191, v109\n\t"
      "v_mfma_f32_16x16x32_bf16 v[56:59], a[8:11], a[244:247], v[56:59]\n\t"
      "buffer_load_dwordx4 a[208:211], v12, s[52:55], s69 offen offset:2048 sc1\n\t"
      "s_add_u32 s69, s69, 0xc00\n\t"
      "v_mfma_f32_16x16x32_bf16 v[60:63], a[16:19], a[244:247], v[60:63]\n\t"
      "ds_read_b128 a[244:247], v34 offset:6144\n\t"
      "v_mul_u32_u24_e32 v146, 24, v145\n\t"
      "v_mfma_f32_16x16x32_bf16 v[64:67], a[0:3], a[248:251], v[64:67]\n\t"
      "v_sub_u32_e32 v144, v144, v146\n\t"
      "v_and_b32_e32 v146, 7, v145\n\t"
      "v_mfma_f32_16x16x32_bf16 v[68:71], a[8:11], a[248:251], v[68:71]\n\t"
      "v_add_u32_e32 v145, s60, v145\n\t"
      "v_xor_b32_e32 v144, v144, v146\n\t"
      "v_mfma_f32_16x16x32_bf16 v[72:75], a[16:19], a[248:251], v[72:75]\n\t"
      "buffer_load_dwordx4 a[196:199], v12, s[52:55], s69 offen sc1\n\t"
      "v_mul_u32_u24_e32 v146, 37, v145\n\t"
      "v_mfma_f32_16x16x32_bf16 v[200:203], v[172:175], a[84:87], 0\n\t"
      "ds_read_b128 a[248:251], v35 offset:6144\n\t"
      "v_lshrrev_b32_e32 v146, 8, v146\n\t"
      "v_mfma_f32_16x16x32_bf16 v[204:207], v[176:179], a[84:87], 0\n\t"
      "v_mul_u32_u24_e32 v147, 7, v146\n\t"
      "v_mul_u32_u24_e32 v146, 0x2a000, v146\n\t"
      "v_mfma_f32_16x16x32_bf16 v[208:211], v[180:183], a[84:87], 0\n\t"
      "s_waitcnt vmcnt(16)\n\t"
      "v_sub_u32_e32 v147, v145, v147\n\t"
      "v_lshl_add_u32 v144, v144, 4, v146\n\t"
      "v_mfma_f32_16x16x32_bf16 v[40:43], a[4:7], a[252:255], v[40:43]\n\t"
      "v_mul_u32_u24_e32 v147, 0x300, v147\n\t"
      "v_cmp_gt_u32_e32 vcc, 48, v145\n\t"
      "v_mfma_f32_16x16x32_bf16 v[44:47], a[12:15], a[252:255], v[44:47]\n\t"
      "v_add_u32_e32 v144, v144, v147\n\t"
      "v_add_u32_e32 v249, s62, v12\n\t"
      "v_mfma_f32_16x16x32_bf16 v[48:51], a[20:23], a[252:255], v[48:51]\n\t"
      "s_waitcnt lgkmcnt(2)\n\t"
      "v_cndmask_b32_e32 v254, v39, v144, vcc\n\t"
      "s_add_u32 s59, s56, 12\n\ts_mul_i32 s60, s59, 43\n\ts_lshr_b32 s60, s60, 8\n\ts_mul_i32 s61, s60, 6\n\ts_sub_u32 s61, s59, s61\n\ts_lshl_b32 s61, s61, 10\n\ts_mul_i32 s62, s60, 0xc000\n\ts_add_u32 s62, s62, s61\n\ts_lshl_b32 s60, s60, 4\n\t"
      "v_mfma_f32_16x16x32_bf16 v[52:55], a[4:7], a[88:91], v[52:55]\n\t"
      "v_add_u32_e32 v144, s61, v12\n\t"
      "v_lshrrev_b32_e32 v144, 4, v144\n\t"
      "v_mfma_f32_16x16x32_bf16 v[56:59], a[12:15], a[88:91], v[56:59]\n\t"
      "v_mul_u32_u24_e32 v145, 0xaab, v144\n\t"
      "v_lshrrev_b32_e32 v145, 16, v145\n\t"
      "v_mfma_f32_16x16x32_bf16 v[60:63], a[20:23], a[88:91], v[60:63]\n\t"
      "v_mul_u32_u24_e32 v146, 24, v145\n\t"
      "v_sub_u32_e32 v144, v144, v146\n\t"
      "v_mfma_f32_16x16x32_bf16 v[64:67], a[4:7], a[92:95], v[64:67]\n\t"
      "v_and_b32_e32 v146, 7, v145\n\t"
      "v_add_u32_e32 v145, s60, v145\n\t"
      "v_mfma_f32_16x16x32_bf16 v[68:71], a[12:15], a[92:95], v[68:71]\n\t"
      "v_xor_b32_e32 v144, v144, v146\n\t"
      "v_mul_u32_u24_e32 v146, 37, v145\n\t"
      "v_mfma_f32_16x16x32_bf16 v[72:75], a[20:23], a[92:95], v[72:75]\n\t"
      "v_lshrrev_b32_e32 v146, 8, v146\n\t"
      "v_mul_u32_u24_e32 v147, 7, v146\n\t"
      "v_smfmac_f32_16x16x64_bf16 v[188:191], v[0:3], a[0:7], v236\n\t"
      "v_sub_u32_e32 v147, v145, v147\n\t"
      "v_mul_u32_u24_e32 v146, 0x2a000, v146\n\t"
      "v_smfmac_f32_16x16x64_bf16 v[192:195], v[0:3], a[8:15], v236\n\t"
      "v_mul_u32_u24_e32 v147, 0x300, v147\n\t"
      "v_lshl_add_u32 v144, v144, 4, v146\n\t"
      "v_smfmac_f32_16x16x64_bf16 v[196:199], v[0:3], a[16:23], v236\n\t"
      "v_cmp_gt_u32_e32 vcc, 48, v145\n\t"
      "v_add_u32_e32 v144, v144, v147\n\t"
      "v_add_u32_e32 v240, 0xc000, v239\n\t"
      "v_add_u32_e32 v244, 0xc000, v243\n\t"
      "v_add_u32_e32 v241, 0x18000, v239\n\t"
      "v_add_u32_e32 v245, 0x18000, v243\n\t"
      "v_add_u32_e32 v250, s62, v12\n\t"
      "v_cndmask_b32_e32 v36, v39, v144, vcc\n\t"
      "v_cvt_pk_bf16_f32 v212, v212, v213\n\t"
      "v_cvt_pk_bf16_f32 v213, v214, v215\n\t"
      "ds_write_b64 v17, v[212:213] offset:24576\n\t"
      "v_mfma_f32_16x16x32_bf16 v[212:215], v[172:175], a[76:79], 0\n\t"
      "v_cvt_pk_bf16_f32 v216, v216, v217\n\t"
      "v_cvt_pk_bf16_f32 v217, v218, v219\n\t"
      "ds_write_b64 v17, v[216:217] offset:24584\n\t"
      "v_mfma_f32_16x16x32_bf16 v[216:219], v[176:179], a[76:79], 0\n\t"
      "v_cvt_pk_bf16_f32 v220, v220, v221\n\t"
      "v_cvt_pk_bf16_f32 v221, v222, v223\n\t"
      "ds_write_b64 v24, v[220:221] offset:24576\n\t"
      "v_mfma_f32_16x16x32_bf16 v[220:223], v[180:183], a[76:79], 0\n\t"
      "s_waitcnt vmcnt(13)\n\t"
      "ds_read_b128 a[252:255], v33 offset:7168\n\t"
      "v_mfma_f32_16x16x32_bf16 v[76:79], a[24:27], a[240:243], 0\n\t"
      "buffer_load_dwordx4 a[204:207], v12, s[52:55], s69 offen offset:1024 sc1\n\t"
      "v_bfe_u32 v5, v237, 2, 2\n\t"
      "v_mfma_f32_16x16x32_bf16 v[80:83], a[32:35], a[240:243], 0\n\t"
      "ds_read_b128 a[88:91], v34 offset:7168\n\t"
      "v_mad_u32_u24 v5, v5, s76, v238\n\t"
      "v_mfma_f32_16x16x32_bf16 v[84:87], a[40:43], a[240:243], 0\n\t"
      "s_waitcnt lgkmcnt(5)\n\t"
      "v_add_u32_e32 v5, 0x180, v5\n\t"
      "v_cvt_pk_bf16_f32 v224, v224, v225\n\t"
      "v_mfma_f32_16x16x32_bf16 v[88:91], a[24:27], a[244:247], 0\n\t"
      "ds_read_b128 a[92:95], v35 offset:7168\n\t"
      "v_cvt_pk_bf16_f32 v225, v226, v227\n\t"
      "v_mfma_f32_16x16x32_bf16 v[92:95], a[32:35], a[244:247], 0\n\t"
      "buffer_load_dwordx4 a[212:215], v12, s[52:55], s69 offen offset:2048 sc1\n\t"
      "s_add_u32 s69, s69, 0xc00\n\t"
      "v_mfma_f32_16x16x32_bf16 v[96:99], a[40:43], a[244:247], 0\n\t"
      "ds_read_b128 a[240:243], v33 offset:8192\n\t"
      "v_cvt_pk_bf16_f32 v228, v228, v229\n\t"
      "v_mfma_f32_16x16x32_bf16 v[100:103], a[24:27], a[248:251], 0\n\t"
      "v_cvt_pk_bf16_f32 v232, v232, v233\n\t"
      "v_cvt_pk_bf16_f32 v229, v230, v231\n\t"
      "v_mfma_f32_16x16x32_bf16 v[104:107], a[32:35], a[248:251], 0\n\t"
      "ds_read_b128 a[244:247], v34 offset:8192\n\t"
      "v_cvt_pk_bf16_f32 v233, v234, v235\n\t"
      "v_mfma_f32_16x16x32_bf16 v[108:111], a[40:43], a[248:251], 0\n\t"
      "s_waitcnt vmcnt(12) lgkmcnt(4)\n\t"
      "ds_read2_b32 v[8:9], v5 offset0:0 offset1:2\n\t"
      "v_mfma_f32_16x16x32_bf16 v[76:79], a[28:31], a[252:255], v[76:79]\n\t"
      "ds_read2_b32 v[10:11], v5 offset0:4 offset1:6\n\t"
      "v_cvt_pk_bf16_f32 v200, v200, v201\n\t"
      "v_mfma_f32_16x16x32_bf16 v[80:83], a[36:39], a[252:255], v[80:83]\n\t"
      "buffer_load_dwordx4 a[216:219], v12, s[52:55], s69 offen sc1\n\t"
      "v_cvt_pk_bf16_f32 v201, v202, v203\n\t"
      "v_mfma_f32_16x16x32_bf16 v[84:87], a[44:47], a[252:255], v[84:87]\n\t"
      "s_waitcnt lgkmcnt(5)\n\t"
      "ds_read_b128 a[248:251], v35 offset:8192\n\t"
      "v_mfma_f32_16x16x32_bf16 v[88:91], a[28:31], a[88:91], v[88:91]\n\t"
      "ds_write_b64 v17, v[224:225] offset:36864\n\t"
      "v_mfma_f32_16x16x32_bf16 v[92:95], a[36:39], a[88:91], v[92:95]\n\t"
      "v_cvt_pk_bf16_f32 v204, v204, v205\n\t"
      "v_cvt_pk_bf16_f32 v205, v206, v207\n\t"
      "v_mfma_f32_16x16x32_bf16 v[96:99], a[44:47], a[88:91], v[96:99]\n\t"
      "s_waitcnt lgkmcnt(6)\n\t"
      "buffer_load_dwordx4 a[224:227], v12, s[52:55], s69 offen offset:1024 sc1\n\t"
      "v_mfma_f32_16x16x32_bf16 v[100:103], a[28:31], a[92:95], v[100:103]\n\t"
      "ds_read_b128 a[252:255], v33 offset:9216\n\t"
      "v_cvt_pk_bf16_f32 v208, v208, v209\n\t"
      "v_mfma_f32_16x16x32_bf16 v[104:107], a[36:39], a[92:95], v[104:107]\n\t"
      "ds_write_b64 v17, v[228:229] offset:36872\n\t"
      "v_mfma_f32_16x16x32_bf16 v[108:111], a[44:47], a[92:95], v[108:111]\n\t"
      "s_waitcnt lgkmcnt(4)\n\t"
      "v_cvt_pk_bf16_f32 v209, v210, v211\n\t"
      "v_cvt_pk_bf16_f32 v212, v212, v213\n\t"
      "v_smfmac_f32_16x16x64_bf16 v[188:191], v[8:11], a[24:31], v236\n\t"
      "buffer_load_dwordx4 a[232:235], v12, s[52:55], s69 offen offset:2048 sc1\n\t"
      "s_add_u32 s69, s69, 0xc00\n\t"
      "v_smfmac_f32_16x16x64_bf16 v[192:195], v[8:11], a[32:39], v236\n\t"
      "ds_read_b128 a[88:91], v34 offset:9216\n\t"
      "v_cvt_pk_bf16_f32 v213, v214, v215\n\t"
      "v_smfmac_f32_16x16x64_bf16 v[196:199], v[8:11], a[40:47], v236\n\t"
      "ds_write_b64 v24, v[232:233] offset:36864\n\t"
      "v_mfma_f32_16x16x32_bf16 v[224:227], v[148:151], a[84:87], 0\n\t"
      "v_cvt_pk_bf16_f32 v216, v216, v217\n\t"
      "v_cvt_pk_bf16_f32 v217, v218, v219\n\t"
      "v_mfma_f32_16x16x32_bf16 v[228:231], v[152:155], a[84:87], 0\n\t"
      "buffer_load_dwordx4 a[220:223], v12, s[52:55], s69 offen sc1\n\t"
      "v_cvt_pk_bf16_f32 v220, v220, v221\n\t"
      "v_mfma_f32_16x16x32_bf16 v[232:235], v[156:159], a[84:87], 0\n\t"
      "s_waitcnt vmcnt(13)\n\t"
      "ds_read_b128 a[92:95], v35 offset:9216\n\t"
      "v_mfma_f32_16x16x32_bf16 v[76:79], a[48:51], a[240:243], v[76:79]\n\t"
      "ds_write_b64 v18, v[200:201] offset:24576\n\t"
      "v_mfma_f32_16x16x32_bf16 v[80:83], a[56:59], a[240:243], v[80:83]\n\t"
      "v_cvt_pk_bf16_f32 v221, v222, v223\n\t"
      "v_cvt_pk_bf16_f32 v224, v224, v225\n\t"
      "v_mfma_f32_16x16x32_bf16 v[84:87], a[64:67], a[240:243], v[84:87]\n\t"
      "buffer_load_dwordx4 a[228:231], v12, s[52:55], s69 offen offset:1024 sc1\n\t"
      "v_cvt_pk_bf16_f32 v225, v226, v227\n\t"
      "v_mfma_f32_16x16x32_bf16 v[88:91], a[48:51], a[244:247], v[88:91]\n\t"
      "ds_read_b128 a[240:243], v33 offset:10240\n\t"
      "v_cvt_pk_bf16_f32 v228, v228, v229\n\t"
      "v_mfma_f32_16x16x32_bf16 v[92:95], a[56:59], a[244:247], v[92:95]\n\t"
      "ds_write_b64 v18, v[204:205] offset:24584\n\t"
      "v_mfma_f32_16x16x32_bf16 v[96:99], a[64:67], a[244:247], v[96:99]\n\t"
      "s_waitcnt lgkmcnt(5)\n\t"
      "v_cvt_pk_bf16_f32 v229, v230, v231\n\t"
      "v_cvt_pk_bf16_f32 v232, v232, v233\n\t"
      "v_mfma_f32_16x16x32_bf16 v[100:103], a[48:51], a[248:251], v[100:103]\n\t"
      "buffer_load_dwordx4 a[236:239], v12, s[52:55], s69 offen offset:2048 sc1\n\t"
      "s_add_u32 s69, s69, 0xc00\n\t"
      "v_mfma_f32_16x16x32_bf16 v[104:107], a[56:59], a[248:251], v[104:107]\n\t"
      "ds_read_b128 a[244:247], v34 offset:10240\n\t"
      "v_cvt_pk_bf16_f32 v233, v234, v235\n\t"
      "v_mfma_f32_16x16x32_bf16 v[108:111], a[64:67], a[248:251], v[108:111]\n\t"
      "ds_write_b64 v25, v[208:209] offset:24576\n\t"
      "v_mfma_f32_16x16x32_bf16 v[200:203], v[148:151], a[76:79], 0\n\t"
      "v_bfe_u32 v4, v237, 4, 2\n\t"
      "v_mfma_f32_16x16x32_bf16 v[204:207], v[152:155], a[76:79], 0\n\t"
      "buffer_load_dwordx4 a[0:3], v12, s[52:55], s69 offen sc1\n\t"
      "v_mad_u32_u24 v4, v4, s76, v238\n\t"
      "v_mfma_f32_16x16x32_bf16 v[208:211], v[156:159], a[76:79], 0\n\t"
      "s_waitcnt vmcnt(13)\n\t"
      "ds_read2_b32 v[0:1], v5 offset0:32 offset1:34\n\t"
      "v_mfma_f32_16x16x32_bf16 v[76:79], a[52:55], a[252:255], v[76:79]\n\t"
      "ds_read2_b32 v[2:3], v5 offset0:36 offset1:38\n\t"
      "v_add_u32_e32 v4, 0x300, v4\n\t"
      "v_mfma_f32_16x16x32_bf16 v[80:83], a[60:63], a[252:255], v[80:83]\n\t"
      "ds_read_b128 a[248:251], v35 offset:10240\n\t"
      "v_cvt_pk_bf16_f32 v200, v200, v201\n\t"
      "v_mfma_f32_16x16x32_bf16 v[84:87], a[68:71], a[252:255], v[84:87]\n\t"
      "ds_read_b128 a[252:255], v33 offset:11264\n\t"
      "ds_read2_b32 v[8:9], v5 offset0:64 offset1:66\n\t"
      "v_mfma_f32_16x16x32_bf16 v[88:91], a[52:55], a[88:91], v[88:91]\n\t"
      "ds_read2_b32 v[10:11], v5 offset0:68 offset1:70\n\t"
      "v_cvt_pk_bf16_f32 v204, v204, v205\n\t"
      "v_mfma_f32_16x16x32_bf16 v[92:95], a[60:63], a[88:91], v[92:95]\n\t"
      "buffer_load_dwordx4 a[8:11], v12, s[52:55], s69 offen offset:1024 sc1\n\t"
      "v_cvt_pk_bf16_f32 v208, v208, v209\n\t"
      "v_mfma_f32_16x16x32_bf16 v[96:99], a[68:71], a[88:91], v[96:99]\n\t"
      "s_waitcnt lgkmcnt(8)\n\t"
      "ds_read_b128 a[88:91], v34 offset:11264\n\t"
      "v_mfma_f32_16x16x32_bf16 v[100:103], a[52:55], a[92:95], v[100:103]\n\t"
      "ds_write_b64 v18, v[212:213] offset:36864\n\t"
      "v_mfma_f32_16x16x32_bf16 v[104:107], a[60:63], a[92:95], v[104:107]\n\t"
      "v_cvt_pk_bf16_f32 v201, v202, v203\n\t"
      "v_cvt_pk_bf16_f32 v205, v206, v207\n\t"
      "v_mfma_f32_16x16x32_bf16 v[108:111], a[68:71], a[92:95], v[108:111]\n\t"
      "s_waitcnt lgkmcnt(6)\n\t"
      "ds_read_b128 a[92:95], v35 offset:11264\n\t"
      "v_smfmac_f32_16x16x64_bf16 v[188:191], v[0:3], a[48:55], v236\n\t"
      "buffer_load_dwordx4 a[16:19], v12, s[52:55], s69 offen offset:2048 sc1\n\t"
      "s_add_u32 s69, s69, 0xc00\n\t"
      "v_smfmac_f32_16x16x64_bf16 v[192:195], v[0:3], a[56:63], v236\n\t"
      "ds_write_b64 v18, v[216:217] offset:36872\n\t"
      "v_smfmac_f32_16x16x64_bf16 v[196:199], v[0:3], a[64:71], v236\n\t"
      "buffer_load_dwordx4 a[4:7], v12, s[52:55], s69 offen sc1\n\t"
      "buffer_load_dwordx4 a[12:15], v12, s[52:55], s69 offen offset:1024 sc1\n\t"
      "ds_read2_b32 v[0:1], v4 offset0:0 offset1:2\n\t"
      "ds_read2_b32 v[2:3], v4 offset0:4 offset1:6\n\t"
      "v_cvt_pk_bf16_f32 v209, v210, v211\n\t"
      "v_bfe_u32 v5, v237, 6, 2\n\t"
      "v_mad_u32_u24 v5, v5, s76, v238\n\t"
      "v_add_u32_e32 v5, 0x480, v5\n\t"
      "s_waitcnt lgkmcnt(3)\n\t"
      "s_barrier\n\t"
      "s_waitcnt vmcnt(14)\n\t"
      "v_mfma_f32_16x16x32_bf16 v[76:79], a[192:195], a[240:243], v[76:79]\n\t"
      "ds_write_b64 v25, v[220:221] offset:36864\n\t"
      "v_mfma_f32_16x16x32_bf16 v[80:83], a[200:203], a[240:243], v[80:83]\n\t"
      "v_mfma_f32_16x16x32_bf16 v[84:87], a[208:211], a[240:243], v[84:87]\n\t"
      "buffer_load_dwordx4 a[20:23], v12, s[52:55], s69 offen offset:2048 sc1\n\t"
      "s_add_u32 s69, s69, 0xc00\n\t"
      "v_mfma_f32_16x16x32_bf16 v[88:91], a[192:195], a[244:247], v[88:91]\n\t"
      "ds_read_b128 a[240:243], v33 offset:12288\n\t"
      "v_mfma_f32_16x16x32_bf16 v[92:95], a[200:203], a[244:247], v[92:95]\n\t"
      "ds_write_b64 v28, v[224:225]\n\t"
      "v_mfma_f32_16x16x32_bf16 v[96:99], a[208:211], a[244:247], v[96:99]\n\t"
      "v_mfma_f32_16x16x32_bf16 v[100:103], a[192:195], a[248:251], v[100:103]\n\t"
      "buffer_load_dwordx4 a[24:27], v12, s[52:55], s69 offen sc1\n\t"
      "v_mfma_f32_16x16x32_bf16 v[104:107], a[200:203], a[248:251], v[104:107]\n\t"
      "ds_read_b128 a[244:247], v34 offset:12288\n\t"
      "v_mfma_f32_16x16x32_bf16 v[108:111], a[208:211], a[248:251], v[108:111]\n\t"
      "ds_write_b64 v28, v[228:229] offset:8\n\t"
      "v_mfma_f32_16x16x32_bf16 v[212:215], a[96:99], a[76:79], 0\n\t"
      "v_mfma_f32_16x16x32_bf16 v[216:219], a[112:115], a[76:79], 0\n\t"
      "buffer_load_dwordx4 a[32:35], v12, s[52:55], s69 offen offset:1024 sc1\n\t"
      "v_mfma_f32_16x16x32_bf16 v[220:223], a[128:131], a[76:79], 0\n\t"
      "s_waitcnt vmcnt(14)\n\t"
      "ds_read_b128 a[248:251], v35 offset:12288\n\t"
      "v_mfma_f32_16x16x32_bf16 v[76:79], a[196:199], a[252:255], v[76:79]\n\t"
      "ds_write_b64 v31, v[232:233]\n\t"
      "v_mfma_f32_16x16x32_bf16 v[80:83], a[204:207], a[252:255], v[80:83]\n\t"
      "v_cvt_pk_bf16_f32 v212, v212, v213\n\t"
      "v_cvt_pk_bf16_f32 v216, v216, v217\n\t"
      "v_mfma_f32_16x16x32_bf16 v[84:87], a[212:215], a[252:255], v[84:87]\n\t"
      "buffer_load_dwordx4 a[40:43], v12, s[52:55], s69 offen offset:2048 sc1\n\t"
      "s_add_u32 s69, s69, 0xc00\n\t"
      "v_mfma_f32_16x16x32_bf16 v[88:91], a[196:199], a[88:91], v[88:91]\n\t"
      "ds_read_b128 a[252:255], v33 offset:13312\n\t"
      "v_cvt_pk_bf16_f32 v220, v220, v221\n\t"
      "v_mfma_f32_16x16x32_bf16 v[92:95], a[204:207], a[88:91], v[92:95]\n\t"
      "ds_write_b64 v29, v[200:201]\n\t"
      "v_mfma_f32_16x16x32_bf16 v[96:99], a[212:215], a[88:91], v[96:99]\n\t"
      "v_cvt_pk_bf16_f32 v213, v214, v215\n\t"
      "v_cvt_pk_bf16_f32 v217, v218, v219\n\t"
      "v_mfma_f32_16x16x32_bf16 v[100:103], a[196:199], a[92:95], v[100:103]\n\t"
      "buffer_load_dwordx4 a[28:31], v12, s[52:55], s69 offen sc1\n\t"
      "v_cvt_pk_bf16_f32 v221, v222, v223\n\t"
      "v_mfma_f32_16x16x32_bf16 v[104:107], a[204:207], a[92:95], v[104:107]\n\t"
      "ds_read_b128 a[88:91], v34 offset:13312\n\t"
      "v_add_f32_e32 v184, v40, v76\n\t"
      "v_mfma_f32_16x16x32_bf16 v[108:111], a[212:215], a[92:95], v[108:111]\n\t"
      "ds_write_b64 v29, v[204:205] offset:8\n\t"
      "v_smfmac_f32_16x16x64_bf16 v[188:191], v[8:11], a[192:199], v236\n\t"
      "v_add_f32_e32 v185, v41, v77\n\t"
      "v_add_f32_e32 v186, v42, v78\n\t"
      "v_smfmac_f32_16x16x64_bf16 v[192:195], v[8:11], a[200:207], v236\n\t"
      "buffer_load_dwordx4 a[36:39], v12, s[52:55], s69 offen offset:1024 sc1\n\t"
      "v_add_f32_e32 v187, v43, v79\n\t"
      "v_smfmac_f32_16x16x64_bf16 v[196:199], v[8:11], a[208:215], v236\n\t"
      "ds_read_b128 a[92:95], v35 offset:13312\n\t"
      "v_sub_f32_e32 v76, v40, v76\n\t"
      "v_mfma_f32_16x16x32_bf16 v[224:227], a[98:101], a[76:79], 0\n\t"
      "ds_write_b64 v32, v[208:209]\n\t"
      "v_mfma_f32_16x16x32_bf16 v[228:231], a[114:117], a[76:79], 0\n\t"
      "v_sub_f32_e32 v77, v41, v77\n\t"
      "v_sub_f32_e32 v78, v42, v78\n\t"
      "v_mfma_f32_16x16x32_bf16 v[232:235], a[130:133], a[76:79], 0\n\t"
      "s_waitcnt vmcnt(14) lgkmcnt(4)\n\t"
      "ds_read2_b32 v[8:9], v4 offset0:32 offset1:34\n\t"
      "v_mfma_f32_16x16x32_bf16 v[112:115], a[216:219], a[240:243], 0\n\t"
      "buffer_load_dwordx4 a[44:47], v12, s[52:55], s69 offen offset:2048 sc1\n\t"
      "s_add_u32 s69, s69, 0xc00\n\t"
      "v_mfma_f32_16x16x32_bf16 v[116:119], a[224:227], a[240:243], 0\n\t"
      "ds_read2_b32 v[10:11], v4 offset0:36 offset1:38\n\t"
      "v_sub_f32_e32 v79, v43, v79\n\t"
      "v_mfma_f32_16x16x32_bf16 v[120:123], a[232:235], a[240:243], 0\n\t"
      "ds_read_b128 a[240:243], v33 offset:14336\n\t"
      "v_add_f32_e32 v40, v44, v80\n\t"
      "v_mfma_f32_16x16x32_bf16 v[124:127], a[216:219], a[244:247], 0\n\t"
      "ds_write_b64 v13, v[212:213]\n\t"
      "v_mfma_f32_16x16x32_bf16 v[128:131], a[224:227], a[244:247], 0\n\t"
      "buffer_load_dwordx4 a[48:51], v12, s[52:55], s69 offen sc1\n\t"
      "v_add_f32_e32 v41, v45, v81\n\t"
      "v_mfma_f32_16x16x32_bf16 v[132:135], a[232:235], a[244:247], 0\n\t"
      "ds_read_b128 a[244:247], v34 offset:14336\n\t"
      "v_add_f32_e32 v42, v46, v82\n\t"
      "v_mfma_f32_16x16x32_bf16 v[136:139], a[216:219], a[248:251], 0\n\t"
      "ds_write_b64 v13, v[216:217] offset:8\n\t"
      "v_mfma_f32_16x16x32_bf16 v[140:143], a[224:227], a[248:251], 0\n\t"
      "v_add_f32_e32 v43, v47, v83\n\t"
      "v_sub_f32_e32 v80, v44, v80\n\t"
      "v_mfma_f32_16x16x32_bf16 v[144:147], a[232:235], a[248:251], 0\n\t"
      "s_waitcnt vmcnt(13)\n\t"
      "buffer_load_dwordx4 a[56:59], v12, s[52:55], s69 offen offset:1024 sc1\n\t"
      "v_mfma_f32_16x16x32_bf16 v[112:115], a[220:223], a[252:255], v[112:115]\n\t"
      "ds_read_b128 a[248:251], v35 offset:14336\n\t"
      "v_sub_f32_e32 v81, v45, v81\n\t"
      "v_mfma_f32_16x16x32_bf16 v[116:119], a[228:231], a[252:255], v[116:119]\n\t"
      "ds_write_b64 v20, v[220:221]\n\t"
      "v_mfma_f32_16x16x32_bf16 v[120:123], a[236:239], a[252:255], v[120:123]\n\t"
      "s_waitcnt lgkmcnt(6)\n\t"
      "v_sub_f32_e32 v82, v46, v82\n\t"
      "v_sub_f32_e32 v83, v47, v83\n\t"
      "v_mfma_f32_16x16x32_bf16 v[124:127], a[220:223], a[88:91], v[124:127]\n\t"
      "buffer_load_dwordx4 a[64:67], v12, s[52:55], s69 offen offset:2048 sc1\n\t"
      "s_add_u32 s69, s69, 0xc00\n\t"
      "v_mfma_f32_16x16x32_bf16 v[128:131], a[228:231], a[88:91], v[128:131]\n\t"
      "ds_read_b128 a[252:255], v33 offset:15360\n\t"
      "v_add_f32_e32 v44, v48, v84\n\t"
      "v_mfma_f32_16x16x32_bf16 v[132:135], a[236:239], a[88:91], v[132:135]\n\t"
      "v_add_f32_e32 v45, v49, v85\n\t"
      "v_add_f32_e32 v46, v50, v86\n\t"
      "v_mfma_f32_16x16x32_bf16 v[136:139], a[220:223], a[92:95], v[136:139]\n\t"
      "ds_read_b128 a[88:91], v34 offset:15360\n\t"
      "v_add_f32_e32 v47, v51, v87\n\t"
      "v_mfma_f32_16x16x32_bf16 v[140:143], a[228:231], a[92:95], v[140:143]\n\t"
      "buffer_load_dwordx4 a[52:55], v12, s[52:55], s69 offen sc1\n\t"
      "v_sub_f32_e32 v84, v48, v84\n\t"
      "v_mfma_f32_16x16x32_bf16 v[144:147], a[236:239], a[92:95], v[144:147]\n\t"
      "ds_read_b128 a[92:95], v35 offset:15360\n\t"
      "v_sub_f32_e32 v85, v49, v85\n\t"
      "v_smfmac_f32_16x16x64_bf16 v[188:191], v[0:3], a[216:223], v236\n\t"
      "v_sub_f32_e32 v86, v50, v86\n\t"
      "v_sub_f32_e32 v87, v51, v87\n\t"
      "v_smfmac_f32_16x16x64_bf16 v[192:195], v[0:3], a[224:231], v236\n\t"
      "v_add_f32_e32 v48, v52, v88\n\t"
      "v_add_f32_e32 v49, v53, v89\n\t"
      "v_smfmac_f32_16x16x64_bf16 v[196:199], v[0:3], a[232:239], v236\n\t"
      "s_waitcnt vmcnt(13) lgkmcnt(4)\n\t"
      "buffer_load_dwordx4 a[60:63], v12, s[52:55], s69 offen offset:1024 sc1\n\t"
      "v_mfma_f32_16x16x32_bf16 v[112:115], a[0:3], a[240:243], v[112:115]\n\t"
      "ds_read2_b32 v[0:1], v4 offset0:64 offset1:66\n\t"
      "v_add_f32_e32 v50, v54, v90\n\t"
      "v_mfma_f32_16x16x32_bf16 v[116:119], a[8:11], a[240:243], v[116:119]\n\t"
      "ds_read2_b32 v[2:3], v4 offset0:68 offset1:70\n\t"
      "v_add_f32_e32 v51, v55, v91\n\t"
      "v_mfma_f32_16x16x32_bf16 v[120:123], a[16:19], a[240:243], v[120:123]\n\t"
      "ds_read_b128 a[240:243], v33 offset:16384\n\t"
      "v_sub_f32_e32 v88, v52, v88\n\t"
      "v_mfma_f32_16x16x32_bf16 v[124:127], a[0:3], a[244:247], v[124:127]\n\t"
      "buffer_load_dwordx4 a[68:71], v12, s[52:55], s69 offen offset:2048 sc1\n\t"
      "s_add_u32 s69, s69, 0xc00\n\t"
      "v_mfma_f32_16x16x32_bf16 v[128:131], a[8:11], a[244:247], v[128:131]\n\t"
      "v_sub_f32_e32 v89, v53, v89\n\t"
      "v_sub_f32_e32 v90, v54, v90\n\t"
      "v_mfma_f32_16x16x32_bf16 v[132:135], a[16:19], a[244:247], v[132:135]\n\t"
      "ds_read_b128 a[244:247], v34 offset:16384\n\t"
      "v_sub_f32_e32 v91, v55, v91\n\t"
      "v_mfma_f32_16x16x32_bf16 v[136:139], a[0:3], a[248:251], v[136:139]\n\t"
      "v_add_f32_e32 v52, v56, v92\n\t"
      "v_add_f32_e32 v53, v57, v93\n\t"
      "v_mfma_f32_16x16x32_bf16 v[140:143], a[8:11], a[248:251], v[140:143]\n\t"
      "buffer_load_dwordx4 a[192:195], v12, s[52:55], s69 offen sc1\n\t"
      "v_add_f32_e32 v54, v58, v94\n\t"
      "v_mfma_f32_16x16x32_bf16 v[144:147], a[16:19], a[248:251], v[144:147]\n\t"
      "s_waitcnt vmcnt(13) lgkmcnt(4)\n\t"
      "ds_read_b128 a[248:251], v35 offset:16384\n\t"
      "v_mfma_f32_16x16x32_bf16 v[112:115], a[4:7], a[252:255], v[112:115]\n\t"
      "v_add_f32_e32 v55, v59, v95\n\t"
      "v_sub_f32_e32 v92, v56, v92\n\t"
      "v_mfma_f32_16x16x32_bf16 v[116:119], a[12:15], a[252:255], v[116:119]\n\t"
      "v_sub_f32_e32 v93, v57, v93\n\t"
      "v_sub_f32_e32 v94, v58, v94\n\t"
      "v_mfma_f32_16x16x32_bf16 v[120:123], a[20:23], a[252:255], v[120:123]\n\t"
      "buffer_load_dwordx4 a[200:203], v12, s[52:55], s69 offen offset:1024 sc1\n\t"
      "v_sub_f32_e32 v95, v59, v95\n\t"
      "v_mfma_f32_16x16x32_bf16 v[124:127], a[4:7], a[88:91], v[124:127]\n\t"
      "ds_read_b128 a[252:255], v33 offset:17408\n\t"
      "v_add_f32_e32 v56, v60, v96\n\t"
      "v_mfma_f32_16x16x32_bf16 v[128:131], a[12:15], a[88:91], v[128:131]\n\t"
      "v_add_f32_e32 v57, v61, v97\n\t"
      "v_add_f32_e32 v58, v62, v98\n\t"
      "v_mfma_f32_16x16x32_bf16 v[132:135], a[20:23], a[88:91], v[132:135]\n\t"
      "ds_read_b128 a[88:91], v34 offset:17408\n\t"
      "v_add_f32_e32 v59, v63, v99\n\t"
      "v_mfma_f32_16x16x32_bf16 v[136:139], a[4:7], a[92:95], v[136:139]\n\t"
      "v_sub_f32_e32 v96, v60, v96\n\t"
      "v_sub_f32_e32 v97, v61, v97\n\t"
      "v_mfma_f32_16x16x32_bf16 v[140:143], a[12:15], a[92:95], v[140:143]\n\t"
      "buffer_load_dwordx4 a[208:211], v12, s[52:55], s69 offen offset:2048 sc1\n\t"
      "s_add_u32 s69, s69, 0xc00\n\t"
      "v_mfma_f32_16x16x32_bf16 v[144:147], a[20:23], a[92:95], v[144:147]\n\t"
      "ds_read_b128 a[92:95], v35 offset:17408\n\t"
      "v_sub_f32_e32 v98, v62, v98\n\t"
      "v_smfmac_f32_16x16x64_bf16 v[188:191], v[8:11], a[0:7], v236\n\t"
      "v_sub_f32_e32 v99, v63, v99\n\t"
      "v_add_f32_e32 v60, v64, v100\n\t"
      "v_smfmac_f32_16x16x64_bf16 v[192:195], v[8:11], a[8:15], v236\n\t"
      "v_add_f32_e32 v61, v65, v101\n\t"
      "v_add_f32_e32 v62, v66, v102\n\t"
      "v_smfmac_f32_16x16x64_bf16 v[196:199], v[8:11], a[16:23], v236\n\t"
      "buffer_load_dwordx4 a[196:199], v12, s[52:55], s69 offen sc1\n\t"
      "v_add_f32_e32 v63, v67, v103\n\t"
      "v_mfma_f32_16x16x32_bf16 v[200:203], a[100:103], a[76:79], 0\n\t"
      "ds_read2_b32 v[8:9], v5 offset0:0 offset1:2\n\t"
      "v_sub_f32_e32 v100, v64, v100\n\t"
      "v_mfma_f32_16x16x32_bf16 v[204:207], a[116:119], a[76:79], 0\n\t"
      "ds_read2_b32 v[10:11], v5 offset0:4 offset1:6\n\t"
      "v_sub_f32_e32 v101, v65, v101\n\t"
      "v_mfma_f32_16x16x32_bf16 v[208:211], a[132:135], a[76:79], 0\n\t"
      "s_waitcnt vmcnt(13) lgkmcnt(4)\n\t"
      "v_sub_f32_e32 v102, v66, v102\n\t"
      "v_sub_f32_e32 v103, v67, v103\n\t"
      "v_mfma_f32_16x16x32_bf16 v[112:115], a[24:27], a[240:243], v[112:115]\n\t"
      "v_add_f32_e32 v64, v68, v104\n\t"
      "v_add_f32_e32 v65, v69, v105\n\t"
      "v_mfma_f32_16x16x32_bf16 v[116:119], a[32:35], a[240:243], v[116:119]\n\t"
      "buffer_load_dwordx4 a[204:207], v12, s[52:55], s69 offen offset:1024 sc1\n\t"
      "v_add_f32_e32 v66, v70, v106\n\t"
      "v_mfma_f32_16x16x32_bf16 v[120:123], a[40:43], a[240:243], v[120:123]\n\t"
      "ds_read_b128 a[240:243], v33 offset:18432\n\t"
      "v_add_f32_e32 v67, v71, v107\n\t"
      "v_mfma_f32_16x16x32_bf16 v[124:127], a[24:27], a[244:247], v[124:127]\n\t"
      "v_cvt_pk_bf16_f32 v224, v224, v225\n\t"
      "v_cvt_pk_bf16_f32 v228, v228, v229\n\t"
      "v_mfma_f32_16x16x32_bf16 v[128:131], a[32:35], a[244:247], v[128:131]\n\t"
      "v_cvt_pk_bf16_f32 v232, v232, v233\n\t"
      "v_sub_f32_e32 v104, v68, v104\n\t"
      "v_mfma_f32_16x16x32_bf16 v[132:135], a[40:43], a[244:247], v[132:135]\n\t"
      "buffer_load_dwordx4 a[212:215], v12, s[52:55], s69 offen offset:2048 sc1\n\t"
      "s_add_u32 s69, s69, 0xc00\n\t"
      "v_mfma_f32_16x16x32_bf16 v[136:139], a[24:27], a[248:251], v[136:139]\n\t"
      "ds_read_b128 a[244:247], v34 offset:18432\n\t"
      "v_sub_f32_e32 v105, v69, v105\n\t"
      "v_mfma_f32_16x16x32_bf16 v[140:143], a[32:35], a[248:251], v[140:143]\n\t"
      "v_sub_f32_e32 v106, v70, v106\n\t"
      "v_sub_f32_e32 v107, v71, v107\n\t"
      "v_mfma_f32_16x16x32_bf16 v[144:147], a[40:43], a[248:251], v[144:147]\n\t"
      "s_waitcnt vmcnt(12)\n\t"
      "ds_read_b128 a[248:251], v35 offset:18432\n\t"
      "v_mfma_f32_16x16x32_bf16 v[112:115], a[28:31], a[252:255], v[112:115]\n\t"
      "buffer_load_dwordx4 a[216:219], v12, s[52:55], s69 offen sc1\n\t"
      "v_cvt_pk_bf16_f32 v225, v226, v227\n\t"
      "v_mfma_f32_16x16x32_bf16 v[116:119], a[36:39], a[252:255], v[116:119]\n\t"
      "v_cvt_pk_bf16_f32 v229, v230, v231\n\t"
      "v_cvt_pk_bf16_f32 v233, v234, v235\n\t"
      "v_mfma_f32_16x16x32_bf16 v[120:123], a[44:47], a[252:255], v[120:123]\n\t"
      "s_waitcnt lgkmcnt(3)\n\t"
      "ds_read_b128 a[252:255], v33 offset:19456\n\t"
      "v_mfma_f32_16x16x32_bf16 v[124:127], a[28:31], a[88:91], v[124:127]\n\t"
      "v_add_f32_e32 v68, v72, v108\n\t"
      "v_add_f32_e32 v69, v73, v109\n\t"
      "v_mfma_f32_16x16x32_bf16 v[128:131], a[36:39], a[88:91], v[128:131]\n\t"
      "buffer_load_dwordx4 a[224:227], v12, s[52:55], s69 offen offset:1024 sc1\n\t"
      "v_add_f32_e32 v70, v74, v110\n\t"
      "v_mfma_f32_16x16x32_bf16 v[132:135], a[44:47], a[88:91], v[132:135]\n\t"
      "v_add_f32_e32 v71, v75, v111\n\t"
      "ds_write_b64 v14, v[224:225]\n\t"
      "v_mfma_f32_16x16x32_bf16 v[136:139], a[28:31], a[92:95], v[136:139]\n\t"
      "ds_write_b64 v14, v[228:229] offset:8\n\t"
      "v_mfma_f32_16x16x32_bf16 v[140:143], a[36:39], a[92:95], v[140:143]\n\t"
      "ds_write_b64 v21, v[232:233]\n\t"
      "v_mfma_f32_16x16x32_bf16 v[144:147], a[44:47], a[92:95], v[144:147]\n\t"
      "v_sub_f32_e32 v108, v72, v108\n\t"
      "v_sub_f32_e32 v109, v73, v109\n\t"
      "v_smfmac_f32_16x16x64_bf16 v[188:191], v[0:3], a[24:31], v236\n\t"
      "v_sub_f32_e32 v110, v74, v110\n\t"
      "v_sub_f32_e32 v111, v75, v111\n\t"
      "v_smfmac_f32_16x16x64_bf16 v[192:195], v[0:3], a[32:39], v236\n\t"
      "buffer_load_dwordx4 a[232:235], v12, s[52:55], s69 offen offset:2048 sc1\n\t"
      "s_add_u32 s69, s69, 0xc00\n\t"
      "v_smfmac_f32_16x16x64_bf16 v[196:199], v[0:3], a[40:47], v236\n\t"
      "ds_read_b128 a[88:91], v34 offset:19456\n\t"
      "v_cvt_pk_bf16_f32 v200, v200, v201\n\t"
      "v_mfma_f32_16x16x32_bf16 v[212:215], a[102:105], a[76:79], 0\n\t"
      "v_cvt_pk_bf16_f32 v201, v202, v203\n\t"
      "v_cvt_pk_bf16_f32 v204, v204, v205\n\t"
      "v_mfma_f32_16x16x32_bf16 v[216:219], a[118:121], a[76:79], 0\n\t"
      "ds_read_b128 a[92:95], v35 offset:19456\n\t"
      "v_cvt_pk_bf16_f32 v205, v206, v207\n\t"
      "v_mfma_f32_16x16x32_bf16 v[220:223], a[134:137], a[76:79], 0\n\t"
      "s_waitcnt vmcnt(12) lgkmcnt(5)\n\t"
      "buffer_load_dwordx4 a[220:223], v12, s[52:55], s69 offen sc1\n\t"
      "v_mfma_f32_16x16x32_bf16 v[148:151], a[48:51], a[240:243], 0\n\t"
      "ds_read2_b32 v[0:1], v5 offset0:32 offset1:34\n\t"
      "v_cvt_pk_bf16_f32 v208, v208, v209\n\t"
      "v_mfma_f32_16x16x32_bf16 v[152:155], a[56:59], a[240:243], 0\n\t"
      "ds_read2_b32 v[2:3], v5 offset0:36 offset1:38\n\t"
      "v_cvt_pk_bf16_f32 v209, v210, v211\n\t"
      "v_mfma_f32_16x16x32_bf16 v[156:159], a[64:67], a[240:243], 0\n\t"
      "ds_read_b128 a[240:243], v33 offset:20480\n\t"
      "v_cvt_pk_bf16_f32 v212, v212, v213\n\t"
      "v_mfma_f32_16x16x32_bf16 v[160:163], a[48:51], a[244:247], 0\n\t"
      "buffer_load_dwordx4 a[228:231], v12, s[52:55], s69 offen offset:1024 sc1\n\t"
      "v_cvt_pk_bf16_f32 v213, v214, v215\n\t"
      "v_mfma_f32_16x16x32_bf16 v[164:167], a[56:59], a[244:247], 0\n\t"
      "ds_write_b64 v15, v[200:201]\n\t"
      "v_mfma_f32_16x16x32_bf16 v[168:171], a[64:67], a[244:247], 0\n\t"
      "v_cvt_pk_bf16_f32 v216, v216, v217\n\t"
      "v_cvt_pk_bf16_f32 v217, v218, v219\n\t"
      "v_mfma_f32_16x16x32_bf16 v[172:175], a[48:51], a[248:251], 0\n\t"
      "ds_read_b128 a[244:247], v34 offset:20480\n\t"
      "v_cvt_pk_bf16_f32 v220, v220, v221\n\t"
      "v_mfma_f32_16x16x32_bf16 v[176:179], a[56:59], a[248:251], 0\n\t"
      "buffer_load_dwordx4 a[236:239], v12, s[52:55], s69 offen offset:2048 sc1\n\t"
      "s_add_u32 s69, s69, 0xc00\n\t"
      "v_mfma_f32_16x16x32_bf16 v[180:183], a[64:67], a[248:251], 0\n\t"
      "s_waitcnt vmcnt(12)\n\t"
      "ds_read_b128 a[248:251], v35 offset:20480\n\t"
      "v_mfma_f32_16x16x32_bf16 v[148:151], a[52:55], a[252:255], v[148:151]\n\t"
      "ds_write_b64 v15, v[204:205] offset:8\n\t"
      "v_mfma_f32_16x16x32_bf16 v[152:155], a[60:63], a[252:255], v[152:155]\n\t"
      "v_cvt_pk_bf16_f32 v221, v222, v223\n\t"
      "v_bfe_u32 v4, v237, 8, 2\n\t"
      "v_mfma_f32_16x16x32_bf16 v[156:159], a[68:71], a[252:255], v[156:159]\n\t"
      "s_waitcnt lgkmcnt(6)\n\t"
      "buffer_load_dwordx4 a[0:3], v12, s[52:55], s69 offen sc1\n\t"
      "v_mfma_f32_16x16x32_bf16 v[160:163], a[52:55], a[88:91], v[160:163]\n\t"
      "ds_read_b128 a[252:255], v33 offset:21504\n\t"
      "v_mad_u32_u24 v4, v4, s76, v238\n\t"
      "v_mfma_f32_16x16x32_bf16 v[164:167], a[60:63], a[88:91], v[164:167]\n\t"
      "ds_write_b64 v22, v[208:209]\n\t"
      "v_mfma_f32_16x16x32_bf16 v[168:171], a[68:71], a[88:91], v[168:171]\n\t"
      "v_add_u32_e32 v4, 0x600, v4\n\t"
      "v_mfma_f32_16x16x32_bf16 v[172:175], a[52:55], a[92:95], v[172:175]\n\t"
      "buffer_load_dwordx4 a[8:11], v12, s[52:55], s69 offen offset:1024 sc1\n\t"
      "v_mfma_f32_16x16x32_bf16 v[176:179], a[60:63], a[92:95], v[176:179]\n\t"
      "ds_read_b128 a[88:91], v34 offset:21504\n\t"
      "v_mfma_f32_16x16x32_bf16 v[180:183], a[68:71], a[92:95], v[180:183]\n\t"
      "ds_write_b64 v16, v[212:213]\n\t"
      "v_smfmac_f32_16x16x64_bf16 v[188:191], v[8:11], a[48:55], v236\n\t"
      "v_smfmac_f32_16x16x64_bf16 v[192:195], v[8:11], a[56:63], v236\n\t"
      "buffer_load_dwordx4 a[16:19], v12, s[52:55], s69 offen offset:2048 sc1\n\t"
      "s_add_u32 s69, s69, 0xc00\n\t"
      "v_smfmac_f32_16x16x64_bf16 v[196:199], v[8:11], a[64:71], v236\n\t"
      "ds_read_b128 a[92:95], v35 offset:21504\n\t"
      "v_mfma_f32_16x16x32_bf16 v[224:227], a[104:107], a[76:79], 0\n\t"
      "ds_write_b64 v16, v[216:217] offset:8\n\t"
      "v_mfma_f32_16x16x32_bf16 v[228:231], a[120:123], a[76:79], 0\n\t"
      "v_mfma_f32_16x16x32_bf16 v[232:235], a[136:139], a[76:79], 0\n\t"
      "s_waitcnt vmcnt(12) lgkmcnt(4)\n\t"
      "buffer_load_dwordx4 a[4:7], v12, s[52:55], s69 offen sc1\n\t"
      "v_mfma_f32_16x16x32_bf16 v[148:151], a[192:195], a[240:243], v[148:151]\n\t"
      "ds_read2_b32 v[8:9], v5 offset0:64 offset1:66\n\t"
      "v_mfma_f32_16x16x32_bf16 v[152:155], a[200:203], a[240:243], v[152:155]\n\t"
      "ds_read2_b32 v[10:11], v5 offset0:68 offset1:70\n\t"
      "v_cvt_pk_bf16_f32 v224, v224, v225\n\t"
      "v_mfma_f32_16x16x32_bf16 v[156:159], a[208:211], a[240:243], v[156:159]\n\t"
      "ds_read_b128 a[240:243], v33 offset:22528\n\t"
      "v_cvt_pk_bf16_f32 v225, v226, v227\n\t"
      "v_mfma_f32_16x16x32_bf16 v[160:163], a[192:195], a[244:247], v[160:163]\n\t"
      "buffer_load_dwordx4 a[12:15], v12, s[52:55], s69 offen offset:1024 sc1\n\t"
      "v_cvt_pk_bf16_f32 v228, v228, v229\n\t"
      "v_mfma_f32_16x16x32_bf16 v[164:167], a[200:203], a[244:247], v[164:167]\n\t"
      "ds_write_b64 v23, v[220:221]\n\t"
      "v_mfma_f32_16x16x32_bf16 v[168:171], a[208:211], a[244:247], v[168:171]\n\t"
      "v_cvt_pk_bf16_f32 v229, v230, v231\n\t"
      "v_cvt_pk_bf16_f32 v232, v232, v233\n\t"
      "v_mfma_f32_16x16x32_bf16 v[172:175], a[192:195], a[248:251], v[172:175]\n\t"
      "ds_read_b128 a[244:247], v34 offset:22528\n\t"
      "v_cvt_pk_bf16_f32 v233, v234, v235\n\t"
      "v_mfma_f32_16x16x32_bf16 v[176:179], a[200:203], a[248:251], v[176:179]\n\t"
      "buffer_load_dwordx4 a[20:23], v12, s[52:55], s69 offen offset:2048 sc1\n\t"
      "s_add_u32 s69, s69, 0xc00\n\t"
      "v_mfma_f32_16x16x32_bf16 v[180:183], a[208:211], a[248:251], v[180:183]\n\t"
      "ds_read_b128 a[248:251], v35 offset:22528\n\t"
      "v_bfe_u32 v5, v237, 10, 2\n\t"
      "v_mfma_f32_16x16x32_bf16 v[200:203], a[106:109], a[76:79], 0\n\t"
      "ds_write_b64 v17, v[224:225]\n\t"
      "v_mfma_f32_16x16x32_bf16 v[204:207], a[122:125], a[76:79], 0\n\t"
      "v_mad_u32_u24 v5, v5, s76, v238\n\t"
      "v_mfma_f32_16x16x32_bf16 v[208:211], a[138:141], a[76:79], 0\n\t"
      "s_waitcnt vmcnt(12)\n\t"
      "buffer_load_dwordx4 a[24:27], v12, s[52:55], s69 offen sc1\n\t"
      "v_mfma_f32_16x16x32_bf16 v[148:151], a[196:199], a[252:255], v[148:151]\n\t"
      "ds_write_b64 v17, v[228:229] offset:8\n\t"
      "v_mfma_f32_16x16x32_bf16 v[152:155], a[204:207], a[252:255], v[152:155]\n\t"
      "v_cvt_pk_bf16_f32 v200, v200, v201\n\t"
      "v_cvt_pk_bf16_f32 v204, v204, v205\n\t"
      "v_mfma_f32_16x16x32_bf16 v[156:159], a[212:215], a[252:255], v[156:159]\n\t"
      "s_waitcnt lgkmcnt(4)\n\t"
      "ds_read_b128 a[252:255], v33 offset:23552\n\t"
      "v_mfma_f32_16x16x32_bf16 v[160:163], a[196:199], a[88:91], v[160:163]\n\t"
      "buffer_load_dwordx4 a[32:35], v12, s[52:55], s69 offen offset:1024 sc1\n\t"
      "v_cvt_pk_bf16_f32 v208, v208, v209\n\t"
      "v_mfma_f32_16x16x32_bf16 v[164:167], a[204:207], a[88:91], v[164:167]\n\t"
      "ds_write_b64 v24, v[232:233]\n\t"
      "v_mfma_f32_16x16x32_bf16 v[168:171], a[212:215], a[88:91], v[168:171]\n\t"
      "ds_read_b128 a[88:91], v34 offset:23552\n\t"
      "v_cvt_pk_bf16_f32 v201, v202, v203\n\t"
      "v_mfma_f32_16x16x32_bf16 v[172:175], a[196:199], a[92:95], v[172:175]\n\t"
      "v_cvt_pk_bf16_f32 v205, v206, v207\n\t"
      "v_cvt_pk_bf16_f32 v209, v210, v211\n\t"
      "v_mfma_f32_16x16x32_bf16 v[176:179], a[204:207], a[92:95], v[176:179]\n\t"
      "buffer_load_dwordx4 a[40:43], v12, s[52:55], s69 offen offset:2048 sc1\n\t"
      "s_add_u32 s69, s69, 0xc00\n\t"
      "v_mfma_f32_16x16x32_bf16 v[180:183], a[212:215], a[92:95], v[180:183]\n\t"
      "ds_read_b128 a[92:95], v35 offset:23552\n\t"
      "v_add_u32_e32 v5, 0x900, v5\n\t"
      "v_smfmac_f32_16x16x64_bf16 v[188:191], v[0:3], a[192:199], v236\n\t"
      "ds_write_b64 v18, v[200:201]\n\t"
      "v_smfmac_f32_16x16x64_bf16 v[192:195], v[0:3], a[200:207], v236\n\t"
      "v_smfmac_f32_16x16x64_bf16 v[196:199], v[0:3], a[208:215], v236\n\t"
      "buffer_load_dwordx4 a[28:31], v12, s[52:55], s69 offen sc1\n\t"
      "buffer_load_dwordx4 a[36:39], v12, s[52:55], s69 offen offset:1024 sc1\n\t"
      "s_waitcnt lgkmcnt(1)\n\t"
      "s_barrier\n\t"
      "s_waitcnt vmcnt(14)\n\t"
      "v_mfma_f32_16x16x32_bf16 v[148:151], a[216:219], a[240:243], v[148:151]\n\t"
      "ds_read2_b32 v[0:1], v4 offset0:0 offset1:2\n\t"
      "v_mfma_f32_16x16x32_bf16 v[152:155], a[224:227], a[240:243], v[152:155]\n\t"
      "ds_read2_b32 v[2:3], v4 offset0:4 offset1:6\n\t"
      "v_mfma_f32_16x16x32_bf16 v[156:159], a[232:235], a[240:243], v[156:159]\n\t"
      "buffer_load_dwordx4 a[44:47], v12, s[52:55], s69 offen offset:2048 sc1\n\t"
      "s_add_u32 s69, s69, 0xc00\n\t"
      "v_mfma_f32_16x16x32_bf16 v[160:163], a[216:219], a[244:247], v[160:163]\n\t"
      "ds_read_b128 a[240:243], v33 offset:24576\n\t"
      "v_mfma_f32_16x16x32_bf16 v[164:167], a[224:227], a[244:247], v[164:167]\n\t"
      "ds_write_b64 v18, v[204:205] offset:8\n\t"
      "v_mfma_f32_16x16x32_bf16 v[168:171], a[232:235], a[244:247], v[168:171]\n\t"
      "v_mfma_f32_16x16x32_bf16 v[172:175], a[216:219], a[248:251], v[172:175]\n\t"
      "buffer_load_dwordx4 a[48:51], v12, s[52:55], s69 offen sc1\n\t"
      "v_mfma_f32_16x16x32_bf16 v[176:179], a[224:227], a[248:251], v[176:179]\n\t"
      "ds_read_b128 a[244:247], v34 offset:24576\n\t"
      "v_mfma_f32_16x16x32_bf16 v[180:183], a[232:235], a[248:251], v[180:183]\n\t"
      "ds_write_b64 v25, v[208:209]\n\t"
      "v_mfma_f32_16x16x32_bf16 v[212:215], a[108:111], a[76:79], 0\n\t"
      "v_mfma_f32_16x16x32_bf16 v[216:219], a[124:127], a[76:79], 0\n\t"
      "buffer_load_dwordx4 a[56:59], v12, s[52:55], s69 offen offset:1024 sc1\n\t"
      "v_mfma_f32_16x16x32_bf16 v[220:223], a[140:143], a[76:79], 0\n\t"
      "s_waitcnt vmcnt(14)\n\t"
      "ds_read_b128 a[248:251], v35 offset:24576\n\t"
      "v_mfma_f32_16x16x32_bf16 v[148:151], a[220:223], a[252:255], v[148:151]\n\t"
      "v_mfma_f32_16x16x32_bf16 v[152:155], a[228:231], a[252:255], v[152:155]\n\t"
      "s_nop 0\n\t"
      "v_cvt_pk_bf16_f32 v212, v212, v213\n\t"
      "v_cvt_pk_bf16_f32 v213, v214, v215\n\t"
      "v_mfma_f32_16x16x32_bf16 v[156:159], a[236:239], a[252:255], v[156:159]\n\t"
      "buffer_load_dwordx4 a[64:67], v12, s[52:55], s69 offen offset:2048 sc1\n\t"
      "s_add_u32 s69, s69, 0xc00\n\t"
      "v_mfma_f32_16x16x32_bf16 v[160:163], a[220:223], a[88:91], v[160:163]\n\t"
      "ds_read_b128 a[252:255], v33 offset:25600\n\t"
      "v_cvt_pk_bf16_f32 v216, v216, v217\n\t"
      "v_mfma_f32_16x16x32_bf16 v[164:167], a[228:231], a[88:91], v[164:167]\n\t"
      "v_cvt_pk_bf16_f32 v220, v220, v221\n\t"
      "v_cvt_pk_bf16_f32 v217, v218, v219\n\t"
      "v_mfma_f32_16x16x32_bf16 v[168:171], a[236:239], a[88:91], v[168:171]\n\t"
      "ds_read_b128 a[88:91], v34 offset:25600\n\t"
      "v_cvt_pk_bf16_f32 v221, v222, v223\n\t"
      "v_mfma_f32_16x16x32_bf16 v[172:175], a[220:223], a[92:95], v[172:175]\n\t"
      "buffer_load_dwordx4 a[52:55], v12, s[52:55], s69 offen sc1\n\t"
      "v_add_f32_e32 v72, v112, v148\n\t"
      "v_mfma_f32_16x16x32_bf16 v[176:179], a[228:231], a[92:95], v[176:179]\n\t"
      "ds_write_b64 v19, v[212:213]\n\t"
      "v_mfma_f32_16x16x32_bf16 v[180:183], a[236:239], a[92:95], v[180:183]\n\t"
      "v_add_f32_e32 v73, v113, v149\n\t"
      "v_add_f32_e32 v74, v114, v150\n\t"
      "v_smfmac_f32_16x16x64_bf16 v[188:191], v[8:11], a[216:223], v236\n\t"
      "ds_read_b128 a[92:95], v35 offset:25600\n\t"
      "v_add_f32_e32 v75, v115, v151\n\t"
      "v_smfmac_f32_16x16x64_bf16 v[192:195], v[8:11], a[224:231], v236\n\t"
      "buffer_load_dwordx4 a[60:63], v12, s[52:55], s69 offen offset:1024 sc1\n\t"
      "v_sub_f32_e32 v148, v112, v148\n\t"
      "v_smfmac_f32_16x16x64_bf16 v[196:199], v[8:11], a[232:239], v236\n\t"
      "ds_read2_b32 v[8:9], v4 offset0:32 offset1:34\n\t"
      "v_sub_f32_e32 v149, v113, v149\n\t"
      "v_mfma_f32_16x16x32_bf16 v[224:227], a[144:147], a[76:79], 0\n\t"
      "ds_read2_b32 v[10:11], v4 offset0:36 offset1:38\n\t"
      "v_sub_f32_e32 v150, v114, v150\n\t"
      "v_mfma_f32_16x16x32_bf16 v[228:231], a[160:163], a[76:79], 0\n\t"
      "v_add_f32_e32 v188, v188, v188\n\t"
      "v_add_f32_e32 v189, v189, v189\n\t"
      "v_mfma_f32_16x16x32_bf16 v[232:235], a[176:179], a[76:79], 0\n\t"
      "s_waitcnt vmcnt(14) lgkmcnt(5)\n\t"
      "buffer_load_dwordx4 a[68:71], v12, s[52:55], s69 offen offset:2048 sc1\n\t"
      "v_mfma_f32_16x16x32_bf16 v[184:187], a[0:3], a[240:243], v[184:187]\n\t"
      "s_add_u32 s69, s69, 0xc00\n\t"
      "v_add_f32_e32 v190, v190, v190\n\t"
      "v_add_f32_e32 v191, v191, v191\n\t"
      "v_mfma_f32_16x16x32_bf16 v[40:43], a[8:11], a[240:243], v[40:43]\n\t"
      "v_add_f32_e32 v192, v192, v192\n\t"
      "v_add_f32_e32 v193, v193, v193\n\t"
      "v_mfma_f32_16x16x32_bf16 v[44:47], a[16:19], a[240:243], v[44:47]\n\t"
      "ds_read_b128 a[240:243], v33 offset:26624\n\t"
      "v_add_f32_e32 v194, v194, v194\n\t"
      "v_mfma_f32_16x16x32_bf16 v[48:51], a[0:3], a[244:247], v[48:51]\n\t"
      "v_add_f32_e32 v195, v195, v195\n\t"
      "v_add_f32_e32 v196, v196, v196\n\t"
      "v_mfma_f32_16x16x32_bf16 v[52:55], a[8:11], a[244:247], v[52:55]\n\t"
      "buffer_load_dwordx4 a[192:195], v12, s[52:55], s69 offen sc1\n\t"
      "v_add_f32_e32 v197, v197, v197\n\t"
      "v_mfma_f32_16x16x32_bf16 v[56:59], a[16:19], a[244:247], v[56:59]\n\t"
      "ds_read_b128 a[244:247], v34 offset:26624\n\t"
      "v_add_f32_e32 v198, v198, v198\n\t"
      "v_mfma_f32_16x16x32_bf16 v[60:63], a[0:3], a[248:251], v[60:63]\n\t"
      "v_add_f32_e32 v199, v199, v199\n\t"
      "v_sub_f32_e32 v151, v115, v151\n\t"
      "v_mfma_f32_16x16x32_bf16 v[64:67], a[8:11], a[248:251], v[64:67]\n\t"
      "ds_write_b64 v19, v[216:217] offset:8\n\t"
      "v_mfma_f32_16x16x32_bf16 v[68:71], a[16:19], a[248:251], v[68:71]\n\t"
      "s_waitcnt vmcnt(13)\n\t"
      "buffer_load_dwordx4 a[200:203], v12, s[52:55], s69 offen offset:1024 sc1\n\t"
      "v_mfma_f32_16x16x32_bf16 v[184:187], a[4:7], a[252:255], v[184:187]\n\t"
      "ds_read_b128 a[248:251], v35 offset:26624\n\t"
      "v_add_f32_e32 v112, v116, v152\n\t"
      "v_mfma_f32_16x16x32_bf16 v[40:43], a[12:15], a[252:255], v[40:43]\n\t"
      "ds_write_b64 v26, v[220:221]\n\t"
      "v_mfma_f32_16x16x32_bf16 v[44:47], a[20:23], a[252:255], v[44:47]\n\t"
      "s_waitcnt lgkmcnt(5)\n\t"
      "v_add_f32_e32 v113, v117, v153\n\t"
      "v_add_f32_e32 v114, v118, v154\n\t"
      "v_mfma_f32_16x16x32_bf16 v[48:51], a[4:7], a[88:91], v[48:51]\n\t"
      "buffer_load_dwordx4 a[208:211], v12, s[52:55], s69 offen offset:2048 sc1\n\t"
      "s_add_u32 s69, s69, 0xc00\n\t"
      "v_mfma_f32_16x16x32_bf16 v[52:55], a[12:15], a[88:91], v[52:55]\n\t"
      "ds_read_b128 a[252:255], v33 offset:27648\n\t"
      "v_add_f32_e32 v115, v119, v155\n\t"
      "v_mfma_f32_16x16x32_bf16 v[56:59], a[20:23], a[88:91], v[56:59]\n\t"
      "v_sub_f32_e32 v152, v116, v152\n\t"
      "v_sub_f32_e32 v153, v117, v153\n\t"
      "v_mfma_f32_16x16x32_bf16 v[60:63], a[4:7], a[92:95], v[60:63]\n\t"
      "ds_read_b128 a[88:91], v34 offset:27648\n\t"
      "v_sub_f32_e32 v154, v118, v154\n\t"
      "v_mfma_f32_16x16x32_bf16 v[64:67], a[12:15], a[92:95], v[64:67]\n\t"
      "buffer_load_dwordx4 a[196:199], v12, s[52:55], s69 offen sc1\n\t"
      "v_sub_f32_e32 v155, v119, v155\n\t"
      "v_mfma_f32_16x16x32_bf16 v[68:71], a[20:23], a[92:95], v[68:71]\n\t"
      "ds_read_b128 a[92:95], v35 offset:27648\n\t"
      "v_add_f32_e32 v116, v120, v156\n\t"
      "v_smfmac_f32_16x16x64_bf16 v[188:191], v[0:3], a[0:7], v236\n\t"
      "v_add_f32_e32 v117, v121, v157\n\t"
      "v_add_f32_e32 v118, v122, v158\n\t"
      "v_smfmac_f32_16x16x64_bf16 v[192:195], v[0:3], a[8:15], v236\n\t"
      "v_add_f32_e32 v119, v123, v159\n\t"
      "v_sub_f32_e32 v156, v120, v156\n\t"
      "v_smfmac_f32_16x16x64_bf16 v[196:199], v[0:3], a[16:23], v236\n\t"
      "s_waitcnt vmcnt(13) lgkmcnt(4)\n\t"
      "buffer_load_dwordx4 a[204:207], v12, s[52:55], s69 offen offset:1024 sc1\n\t"
      "v_mfma_f32_16x16x32_bf16 v[184:187], a[24:27], a[240:243], v[184:187]\n\t"
      "ds_read2_b32 v[0:1], v4 offset0:64 offset1:66\n\t"
      "v_sub_f32_e32 v157, v121, v157\n\t"
      "v_mfma_f32_16x16x32_bf16 v[40:43], a[32:35], a[240:243], v[40:43]\n\t"
      "ds_read2_b32 v[2:3], v4 offset0:68 offset1:70\n\t"
      "v_sub_f32_e32 v158, v122, v158\n\t"
      "v_mfma_f32_16x16x32_bf16 v[44:47], a[40:43], a[240:243], v[44:47]\n\t"
      "ds_read_b128 a[240:243], v33 offset:28672\n\t"
      "v_sub_f32_e32 v159, v123, v159\n\t"
      "v_mfma_f32_16x16x32_bf16 v[48:51], a[24:27], a[244:247], v[48:51]\n\t"
      "buffer_load_dwordx4 a[212:215], v12, s[52:55], s69 offen offset:2048 sc1\n\t"
      "s_add_u32 s69, s69, 0xc00\n\t"
      "v_mfma_f32_16x16x32_bf16 v[52:55], a[32:35], a[244:247], v[52:55]\n\t"
      "v_add_f32_e32 v120, v124, v160\n\t"
      "v_add_f32_e32 v121, v125, v161\n\t"
      "v_mfma_f32_16x16x32_bf16 v[56:59], a[40:43], a[244:247], v[56:59]\n\t"
      "ds_read_b128 a[244:247], v34 offset:28672\n\t"
      "v_add_f32_e32 v122, v126, v162\n\t"
      "v_mfma_f32_16x16x32_bf16 v[60:63], a[24:27], a[248:251], v[60:63]\n\t"
      "v_add_f32_e32 v123, v127, v163\n\t"
      "v_sub_f32_e32 v160, v124, v160\n\t"
      "v_mfma_f32_16x16x32_bf16 v[64:67], a[32:35], a[248:251], v[64:67]\n\t"
      "buffer_load_dwordx4 a[216:219], v12, s[52:55], s69 offen sc1\n\t"
      "v_sub_f32_e32 v161, v125, v161\n\t"
      "v_mfma_f32_16x16x32_bf16 v[68:71], a[40:43], a[248:251], v[68:71]\n\t"
      "s_waitcnt vmcnt(13) lgkmcnt(4)\n\t"
      "ds_read_b128 a[248:251], v35 offset:28672\n\t"
      "v_mfma_f32_16x16x32_bf16 v[184:187], a[28:31], a[252:255], v[184:187]\n\t"
      "v_sub_f32_e32 v162, v126, v162\n\t"
      "v_sub_f32_e32 v163, v127, v163\n\t"
      "v_mfma_f32_16x16x32_bf16 v[40:43], a[36:39], a[252:255], v[40:43]\n\t"
      "v_add_f32_e32 v124, v128, v164\n\t"
      "v_add_f32_e32 v125, v129, v165\n\t"
      "v_mfma_f32_16x16x32_bf16 v[44:47], a[44:47], a[252:255], v[44:47]\n\t"
      "buffer_load_dwordx4 a[224:227], v12, s[52:55], s69 offen offset:1024 sc1\n\t"
      "v_add_f32_e32 v126, v130, v166\n\t"
      "v_mfma_f32_16x16x32_bf16 v[48:51], a[28:31], a[88:91], v[48:51]\n\t"
      "ds_read_b128 a[252:255], v33 offset:29696\n\t"
      "v_add_f32_e32 v127, v131, v167\n\t"
      "v_mfma_f32_16x16x32_bf16 v[52:55], a[36:39], a[88:91], v[52:55]\n\t"
      "v_sub_f32_e32 v164, v128, v164\n\t"
      "v_sub_f32_e32 v165, v129, v165\n\t"
      "v_mfma_f32_16x16x32_bf16 v[56:59], a[44:47], a[88:91], v[56:59]\n\t"
      "ds_read_b128 a[88:91], v34 offset:29696\n\t"
      "v_sub_f32_e32 v166, v130, v166\n\t"
      "v_mfma_f32_16x16x32_bf16 v[60:63], a[28:31], a[92:95], v[60:63]\n\t"
      "buffer_load_dwordx4 a[232:235], v12, s[52:55], s69 offen offset:2048 sc1\n\t"
      "s_add_u32 s69, s69, 0xc00\n\t"
      "v_mfma_f32_16x16x32_bf16 v[64:67], a[36:39], a[92:95], v[64:67]\n\t"
      "v_sub_f32_e32 v167, v131, v167\n\t"
      "v_add_f32_e32 v128, v132, v168\n\t"
      "v_mfma_f32_16x16x32_bf16 v[68:71], a[44:47], a[92:95], v[68:71]\n\t"
      "ds_read_b128 a[92:95], v35 offset:29696\n\t"
      "v_add_f32_e32 v129, v133, v169\n\t"
      "v_smfmac_f32_16x16x64_bf16 v[188:191], v[8:11], a[24:31], v236\n\t"
      "v_add_f32_e32 v130, v134, v170\n\t"
      "v_add_f32_e32 v131, v135, v171\n\t"
      "v_smfmac_f32_16x16x64_bf16 v[192:195], v[8:11], a[32:39], v236\n\t"
      "v_sub_f32_e32 v168, v132, v168\n\t"
      "v_sub_f32_e32 v169, v133, v169\n\t"
      "v_smfmac_f32_16x16x64_bf16 v[196:199], v[8:11], a[40:47], v236\n\t"
      "buffer_load_dwordx4 a[220:223], v12, s[52:55], s69 offen sc1\n\t"
      "v_sub_f32_e32 v170, v134, v170\n\t"
      "v_mfma_f32_16x16x32_bf16 v[200:203], a[146:149], a[76:79], 0\n\t"
      "ds_read2_b32 v[8:9], v5 offset0:0 offset1:2\n\t"
      "v_sub_f32_e32 v171, v135, v171\n\t"
      "v_mfma_f32_16x16x32_bf16 v[204:207], a[162:165], a[76:79], 0\n\t"
      "ds_read2_b32 v[10:11], v5 offset0:4 offset1:6\n\t"
      "v_add_f32_e32 v132, v136, v172\n\t"
      "v_mfma_f32_16x16x32_bf16 v[208:211], a[178:181], a[76:79], 0\n\t"
      "s_waitcnt vmcnt(13) lgkmcnt(4)\n\t"
      "v_add_f32_e32 v133, v137, v173\n\t"
      "v_add_f32_e32 v134, v138, v174\n\t"
      "v_mfma_f32_16x16x32_bf16 v[184:187], a[48:51], a[240:243], v[184:187]\n\t"
      "v_add_f32_e32 v135, v139, v175\n\t"
      "v_sub_f32_e32 v172, v136, v172\n\t"
      "v_mfma_f32_16x16x32_bf16 v[40:43], a[56:59], a[240:243], v[40:43]\n\t"
      "buffer_load_dwordx4 a[228:231], v12, s[52:55], s69 offen offset:1024 sc1\n\t"
      "v_sub_f32_e32 v173, v137, v173\n\t"
      "v_mfma_f32_16x16x32_bf16 v[44:47], a[64:67], a[240:243], v[44:47]\n\t"
      "ds_read_b128 a[240:243], v33 offset:36864\n\t"
      "v_sub_f32_e32 v174, v138, v174\n\t"
      "v_mfma_f32_16x16x32_bf16 v[48:51], a[48:51], a[244:247], v[48:51]\n\t"
      "v_sub_f32_e32 v175, v139, v175\n\t"
      "v_add_f32_e32 v136, v140, v176\n\t"
      "v_mfma_f32_16x16x32_bf16 v[52:55], a[56:59], a[244:247], v[52:55]\n\t"
      "v_add_f32_e32 v137, v141, v177\n\t"
      "v_add_f32_e32 v138, v142, v178\n\t"
      "v_mfma_f32_16x16x32_bf16 v[56:59], a[64:67], a[244:247], v[56:59]\n\t"
      "buffer_load_dwordx4 a[236:239], v12, s[52:55], s69 offen offset:2048 sc1\n\t"
      "s_add_u32 s69, s69, 0xc00\n\t"
      "v_mfma_f32_16x16x32_bf16 v[60:63], a[48:51], a[248:251], v[60:63]\n\t"
      "ds_read_b128 a[244:247], v34 offset:36864\n\t"
      "v_add_f32_e32 v139, v143, v179\n\t"
      "v_mfma_f32_16x16x32_bf16 v[64:67], a[56:59], a[248:251], v[64:67]\n\t"
      "v_cvt_pk_bf16_f32 v224, v224, v225\n\t"
      "v_cvt_pk_bf16_f32 v228, v228, v229\n\t"
      "v_mfma_f32_16x16x32_bf16 v[68:71], a[64:67], a[248:251], v[68:71]\n\t"
      "s_waitcnt vmcnt(12)\n\t"
      "ds_read_b128 a[248:251], v35 offset:36864\n\t"
      "v_mfma_f32_16x16x32_bf16 v[184:187], a[52:55], a[252:255], v[184:187]\n\t"
      "buffer_load_dwordx4 a[0:3], v12, s[52:55], s69 offen sc1\n\t"
      "v_cvt_pk_bf16_f32 v232, v232, v233\n\t"
      "v_mfma_f32_16x16x32_bf16 v[40:43], a[60:63], a[252:255], v[40:43]\n\t"
      "v_sub_f32_e32 v176, v140, v176\n\t"
      "v_sub_f32_e32 v177, v141, v177\n\t"
      "v_mfma_f32_16x16x32_bf16 v[44:47], a[68:71], a[252:255], v[44:47]\n\t"
      "s_waitcnt lgkmcnt(3)\n\t"
      "ds_read_b128 a[252:255], v33 offset:37888\n\t"
      "v_mfma_f32_16x16x32_bf16 v[48:51], a[52:55], a[88:91], v[48:51]\n\t"
      "v_sub_f32_e32 v178, v142, v178\n\t"
      "v_sub_f32_e32 v179, v143, v179\n\t"
      "v_mfma_f32_16x16x32_bf16 v[52:55], a[60:63], a[88:91], v[52:55]\n\t"
      "v_cvt_pk_bf16_f32 v225, v226, v227\n\t"
      "v_cvt_pk_bf16_f32 v229, v230, v231\n\t"
      "v_mfma_f32_16x16x32_bf16 v[56:59], a[68:71], a[88:91], v[56:59]\n\t"
      "v_cvt_pk_bf16_f32 v233, v234, v235\n\t"
      "v_add_f32_e32 v140, v144, v180\n\t"
      "v_mfma_f32_16x16x32_bf16 v[60:63], a[52:55], a[92:95], v[60:63]\n\t"
      "v_add_f32_e32 v141, v145, v181\n\t"
      "v_add_f32_e32 v142, v146, v182\n\t"
      "v_mfma_f32_16x16x32_bf16 v[64:67], a[60:63], a[92:95], v[64:67]\n\t"
      "v_add_f32_e32 v143, v147, v183\n\t"
      "ds_write_b64 v13, v[224:225] offset:12288\n\t"
      "v_mfma_f32_16x16x32_bf16 v[68:71], a[68:71], a[92:95], v[68:71]\n\t"
      "ds_write_b64 v13, v[228:229] offset:12296\n\t"
      "v_smfmac_f32_16x16x64_bf16 v[188:191], v[0:3], a[48:55], v236\n\t"
      "ds_write_b64 v20, v[232:233] offset:12288\n\t"
      "v_smfmac_f32_16x16x64_bf16 v[192:195], v[0:3], a[56:63], v236\n\t"
      "v_sub_f32_e32 v180, v144, v180\n\t"
      "v_sub_f32_e32 v181, v145, v181\n\t"
      "v_smfmac_f32_16x16x64_bf16 v[196:199], v[0:3], a[64:71], v236\n\t"
      "v_sub_f32_e32 v182, v146, v182\n\t"
      "v_sub_f32_e32 v183, v147, v183\n\t"
      "v_mfma_f32_16x16x32_bf16 v[212:215], a[148:151], a[76:79], 0\n\t"
      "buffer_load_dwordx4 a[8:11], v12, s[52:55], s69 offen offset:1024 sc1\n\t"
      "v_cvt_pk_bf16_f32 v200, v200, v201\n\t"
      "v_mfma_f32_16x16x32_bf16 v[216:219], a[164:167], a[76:79], 0\n\t"
      "ds_read_b128 a[88:91], v34 offset:37888\n\t"
      "v_cvt_pk_bf16_f32 v201, v202, v203\n\t"
      "v_mfma_f32_16x16x32_bf16 v[220:223], a[180:183], a[76:79], 0\n\t"
      "s_waitcnt vmcnt(11) lgkmcnt(4)\n\t"
      "v_cvt_pk_bf16_f32 v204, v204, v205\n\t"
      "v_cvt_pk_bf16_f32 v205, v206, v207\n\t"
      "v_mfma_f32_16x16x32_bf16 v[72:75], a[192:195], a[240:243], v[72:75]\n\t"
      "ds_read_b128 a[92:95], v35 offset:37888\n\t"
      "buffer_load_dwordx4 a[16:19], v12, s[52:55], s69 offen offset:2048 sc1\n\t"
      "v_mfma_f32_16x16x32_bf16 v[112:115], a[200:203], a[240:243], v[112:115]\n\t"
      "s_add_u32 s69, s69, 0xc00\n\t"
      "v_cvt_pk_bf16_f32 v208, v208, v209\n\t"
      "v_cvt_pk_bf16_f32 v209, v210, v211\n\t"
      "v_mfma_f32_16x16x32_bf16 v[116:119], a[208:211], a[240:243], v[116:119]\n\t"
      "ds_read_b128 a[240:243], v33 offset:38912\n\t"
      "v_cvt_pk_bf16_f32 v212, v212, v213\n\t"
      "v_mfma_f32_16x16x32_bf16 v[120:123], a[192:195], a[244:247], v[120:123]\n\t"
      "ds_write_b64 v14, v[200:201] offset:12288\n\t"
      "v_mfma_f32_16x16x32_bf16 v[124:127], a[200:203], a[244:247], v[124:127]\n\t"
      "buffer_load_dwordx4 a[4:7], v12, s[52:55], s69 offen sc1\n\t"
      "v_cvt_pk_bf16_f32 v213, v214, v215\n\t"
      "v_mfma_f32_16x16x32_bf16 v[128:131], a[208:211], a[244:247], v[128:131]\n\t"
      "ds_read_b128 a[244:247], v34 offset:38912\n\t"
      "v_cvt_pk_bf16_f32 v216, v216, v217\n\t"
      "v_mfma_f32_16x16x32_bf16 v[132:135], a[192:195], a[248:251], v[132:135]\n\t"
      "ds_write_b64 v14, v[204:205] offset:12296\n\t"
      "v_mfma_f32_16x16x32_bf16 v[136:139], a[200:203], a[248:251], v[136:139]\n\t"
      "v_cvt_pk_bf16_f32 v217, v218, v219\n\t"
      "v_cvt_pk_bf16_f32 v220, v220, v221\n\t"
      "v_mfma_f32_16x16x32_bf16 v[140:143], a[208:211], a[248:251], v[140:143]\n\t"
      "s_waitcnt vmcnt(10)\n\t"
      "buffer_load_dwordx4 a[12:15], v12, s[52:55], s69 offen offset:1024 sc1\n\t"
      "v_mfma_f32_16x16x32_bf16 v[72:75], a[196:199], a[252:255], v[72:75]\n\t"
      "ds_read_b128 a[248:251], v35 offset:38912\n\t"
      "v_cvt_pk_bf16_f32 v221, v222, v223\n\t"
      "v_mfma_f32_16x16x32_bf16 v[112:115], a[204:207], a[252:255], v[112:115]\n\t"
      "ds_write_b64 v21, v[208:209] offset:12288\n\t"
      "v_mfma_f32_16x16x32_bf16 v[116:119], a[212:215], a[252:255], v[116:119]\n\t"
      "s_waitcnt lgkmcnt(6)\n\t"
      "buffer_load_dwordx4 a[20:23], v12, s[52:55], s69 offen offset:2048 sc1\n\t"
      "v_mfma_f32_16x16x32_bf16 v[120:123], a[196:199], a[88:91], v[120:123]\n\t"
      "s_add_u32 s69, s69, 0xc00\n\t"
      "ds_read_b128 a[252:255], v33 offset:39936\n\t"
      "v_mfma_f32_16x16x32_bf16 v[124:127], a[204:207], a[88:91], v[124:127]\n\t"
      "ds_write_b64 v15, v[212:213] offset:12288\n\t"
      "v_mfma_f32_16x16x32_bf16 v[128:131], a[212:215], a[88:91], v[128:131]\n\t"
      "v_bfe_u32 v4, v237, 12, 2\n\t"
      "v_mfma_f32_16x16x32_bf16 v[132:135], a[196:199], a[92:95], v[132:135]\n\t"
      "buffer_load_dwordx4 a[24:27], v12, s[52:55], s69 offen sc1\n\t"
      "v_mad_u32_u24 v4, v4, s76, v238\n\t"
      "v_mfma_f32_16x16x32_bf16 v[136:139], a[204:207], a[92:95], v[136:139]\n\t"
      "ds_read_b128 a[88:91], v34 offset:39936\n\t"
      "v_add_u32_e32 v4, 0x780, v4\n\t"
      "v_mfma_f32_16x16x32_bf16 v[140:143], a[212:215], a[92:95], v[140:143]\n\t"
      "ds_write_b64 v15, v[216:217] offset:12296\n\t"
      "v_smfmac_f32_16x16x64_bf16 v[188:191], v[8:11], a[192:199], v236\n\t"
      "v_smfmac_f32_16x16x64_bf16 v[192:195], v[8:11], a[200:207], v236\n\t"
      "buffer_load_dwordx4 a[32:35], v12, s[52:55], s69 offen offset:1024 sc1\n\t"
      "v_smfmac_f32_16x16x64_bf16 v[196:199], v[8:11], a[208:215], v236\n\t"
      "ds_read_b128 a[92:95], v35 offset:39936\n\t"
      "v_mfma_f32_16x16x32_bf16 v[224:227], a[150:153], a[76:79], 0\n\t"
      "ds_write_b64 v22, v[220:221] offset:12288\n\t"
      "v_mfma_f32_16x16x32_bf16 v[228:231], a[166:169], a[76:79], 0\n\t"
      "v_mfma_f32_16x16x32_bf16 v[232:235], a[182:185], a[76:79], 0\n\t"
      "s_waitcnt vmcnt(11) lgkmcnt(4)\n\t"
      "buffer_load_dwordx4 a[40:43], v12, s[52:55], s69 offen offset:2048 sc1\n\t"
      "v_mfma_f32_16x16x32_bf16 v[72:75], a[216:219], a[240:243], v[72:75]\n\t"
      "s_add_u32 s69, s69, 0xc00\n\t"
      "ds_read2_b32 v[0:1], v5 offset0:32 offset1:34\n\t"
      "v_mfma_f32_16x16x32_bf16 v[112:115], a[224:227], a[240:243], v[112:115]\n\t"
      "ds_read2_b32 v[2:3], v5 offset0:36 offset1:38\n\t"
      "v_cvt_pk_bf16_f32 v224, v224, v225\n\t"
      "v_mfma_f32_16x16x32_bf16 v[116:119], a[232:235], a[240:243], v[116:119]\n\t"
      "ds_read_b128 a[240:243], v33 offset:40960\n\t"
      "v_cvt_pk_bf16_f32 v228, v228, v229\n\t"
      "v_mfma_f32_16x16x32_bf16 v[120:123], a[216:219], a[244:247], v[120:123]\n\t"
      "buffer_load_dwordx4 a[28:31], v12, s[52:55], s69 offen sc1\n\t"
      "v_cvt_pk_bf16_f32 v232, v232, v233\n\t"
      "v_mfma_f32_16x16x32_bf16 v[124:127], a[224:227], a[244:247], v[124:127]\n\t"
      "ds_read2_b32 v[8:9], v5 offset0:64 offset1:66\n\t"
      "v_cvt_pk_bf16_f32 v225, v226, v227\n\t"
      "v_mfma_f32_16x16x32_bf16 v[128:131], a[232:235], a[244:247], v[128:131]\n\t"
      "ds_read_b128 a[244:247], v34 offset:40960\n\t"
      "v_cvt_pk_bf16_f32 v229, v230, v231\n\t"
      "v_mfma_f32_16x16x32_bf16 v[132:135], a[216:219], a[248:251], v[132:135]\n\t"
      "v_cvt_pk_bf16_f32 v233, v234, v235\n\t"
      "v_mfma_f32_16x16x32_bf16 v[136:139], a[224:227], a[248:251], v[136:139]\n\t"
      "ds_read2_b32 v[10:11], v5 offset0:68 offset1:70\n\t"
      "v_bfe_u32 v5, v237, 14, 2\n\t"
      "v_mfma_f32_16x16x32_bf16 v[140:143], a[232:235], a[248:251], v[140:143]\n\t"
      "ds_read_b128 a[248:251], v35 offset:40960\n\t"
      "buffer_load_dwordx4 a[36:39], v12, s[52:55], s69 offen offset:1024 sc1\n\t"
      "v_mfma_f32_16x16x32_bf16 v[200:203], a[152:155], a[76:79], 0\n\t"
      "ds_write_b64 v16, v[224:225] offset:12288\n\t"
      "v_mfma_f32_16x16x32_bf16 v[204:207], a[168:171], a[76:79], 0\n\t"
      "v_mad_u32_u24 v5, v5, s76, v238\n\t"
      "v_mfma_f32_16x16x32_bf16 v[208:211], a[184:187], a[76:79], 0\n\t"
      "s_waitcnt vmcnt(11)\n\t"
      "v_add_u32_e32 v5, 0xa80, v5\n\t"
      "v_mfma_f32_16x16x32_bf16 v[72:75], a[220:223], a[252:255], v[72:75]\n\t"
      "buffer_load_dwordx4 a[44:47], v12, s[52:55], s69 offen offset:2048 sc1\n\t"
      "s_add_u32 s69, s69, 0xc00\n\t"
      "v_mfma_f32_16x16x32_bf16 v[112:115], a[228:231], a[252:255], v[112:115]\n\t"
      "ds_write_b64 v16, v[228:229] offset:12296\n\t"
      "v_mfma_f32_16x16x32_bf16 v[116:119], a[236:239], a[252:255], v[116:119]\n\t"
      "s_waitcnt lgkmcnt(5)\n\t"
      "v_cvt_pk_bf16_f32 v200, v200, v201\n\t"
      "v_cvt_pk_bf16_f32 v204, v204, v205\n\t"
      "v_mfma_f32_16x16x32_bf16 v[120:123], a[220:223], a[88:91], v[120:123]\n\t"
      "ds_read_b128 a[252:255], v33 offset:41984\n\t"
      "v_cvt_pk_bf16_f32 v208, v208, v209\n\t"
      "v_mfma_f32_16x16x32_bf16 v[124:127], a[228:231], a[88:91], v[124:127]\n\t"
      "buffer_load_dwordx4 a[48:51], v12, s[52:55], s69 offen sc1\n\t"
      "v_cvt_pk_bf16_f32 v201, v202, v203\n\t"
      "v_mfma_f32_16x16x32_bf16 v[128:131], a[236:239], a[88:91], v[128:131]\n\t"
      "ds_read_b128 a[88:91], v34 offset:41984\n\t"
      "v_cvt_pk_bf16_f32 v205, v206, v207\n\t"
      "v_mfma_f32_16x16x32_bf16 v[132:135], a[220:223], a[92:95], v[132:135]\n\t"
      "ds_write_b64 v23, v[232:233] offset:12288\n\t"
      "v_mfma_f32_16x16x32_bf16 v[136:139], a[228:231], a[92:95], v[136:139]\n\t"
      "v_cvt_pk_bf16_f32 v209, v210, v211\n\t"
      "buffer_load_dwordx4 a[56:59], v12, s[52:55], s69 offen offset:1024 sc1\n\t"
      "v_mfma_f32_16x16x32_bf16 v[140:143], a[236:239], a[92:95], v[140:143]\n\t"
      "ds_read_b128 a[92:95], v35 offset:41984\n\t"
      "v_smfmac_f32_16x16x64_bf16 v[188:191], v[0:3], a[216:223], v236\n\t"
      "ds_write_b64 v17, v[200:201] offset:12288\n\t"
      "v_smfmac_f32_16x16x64_bf16 v[192:195], v[0:3], a[224:231], v236\n\t"
      "v_smfmac_f32_16x16x64_bf16 v[196:199], v[0:3], a[232:239], v236\n\t"
      "s_waitcnt vmcnt(11)\n\t"
      "buffer_load_dwordx4 a[64:67], v12, s[52:55], s69 offen offset:2048 sc1\n\t"
      "v_mfma_f32_16x16x32_bf16 v[72:75], a[0:3], a[240:243], v[72:75]\n\t"
      "s_add_u32 s69, s69, 0xc00\n\t"
      "ds_read2_b32 v[0:1], v4 offset0:0 offset1:2\n\t"
      "v_mfma_f32_16x16x32_bf16 v[112:115], a[8:11], a[240:243], v[112:115]\n\t"
      "ds_read2_b32 v[2:3], v4 offset0:4 offset1:6\n\t"
      "v_mfma_f32_16x16x32_bf16 v[116:119], a[16:19], a[240:243], v[116:119]\n\t"
      "s_waitcnt lgkmcnt(6)\n\t"
      "ds_read_b128 a[240:243], v33 offset:30720\n\t"
      "v_mfma_f32_16x16x32_bf16 v[120:123], a[0:3], a[244:247], v[120:123]\n\t"
      "buffer_load_dwordx4 a[52:55], v12, s[52:55], s69 offen sc1\n\t"
      "v_mfma_f32_16x16x32_bf16 v[124:127], a[8:11], a[244:247], v[124:127]\n\t"
      "ds_write_b64 v17, v[204:205] offset:12296\n\t"
      "v_mfma_f32_16x16x32_bf16 v[128:131], a[16:19], a[244:247], v[128:131]\n\t"
      "v_mfma_f32_16x16x32_bf16 v[132:135], a[0:3], a[248:251], v[132:135]\n\t"
      "ds_read_b128 a[244:247], v34 offset:30720\n\t"
      "v_mfma_f32_16x16x32_bf16 v[136:139], a[8:11], a[248:251], v[136:139]\n\t"
      "buffer_load_dwordx4 a[60:63], v12, s[52:55], s69 offen offset:1024 sc1\n\t"
      "v_mfma_f32_16x16x32_bf16 v[140:143], a[16:19], a[248:251], v[140:143]\n\t"
      "ds_read_b128 a[248:251], v35 offset:30720\n\t"
      "v_mfma_f32_16x16x32_bf16 v[212:215], a[154:157], a[76:79], 0\n\t"
      "ds_write_b64 v24, v[208:209] offset:12288\n\t"
      "v_mfma_f32_16x16x32_bf16 v[216:219], a[170:173], a[76:79], 0\n\t"
      "buffer_load_dwordx4 a[68:71], v12, s[52:55], s69 offen offset:2048 sc1\n\t"
      "s_add_u32 s69, s69, 0xc00\n\t"
      "v_mfma_f32_16x16x32_bf16 v[220:223], a[186:189], a[76:79], 0\n\t"
      "s_waitcnt vmcnt(12)\n\t"
      "v_mfma_f32_16x16x32_bf16 v[72:75], a[4:7], a[252:255], v[72:75]\n\t"
      "s_nop 0\n\t"
      "v_cvt_pk_bf16_f32 v212, v212, v213\n\t"
      "v_cvt_pk_bf16_f32 v213, v214, v215\n\t"
      "v_mfma_f32_16x16x32_bf16 v[112:115], a[12:15], a[252:255], v[112:115]\n\t"
      "v_cvt_pk_bf16_f32 v216, v216, v217\n\t"
      "v_cvt_pk_bf16_f32 v217, v218, v219\n\t"
      "v_mfma_f32_16x16x32_bf16 v[116:119], a[20:23], a[252:255], v[116:119]\n\t"
      "s_waitcnt lgkmcnt(4)\n\t"
      "ds_read_b128 a[252:255], v33 offset:31744\n\t"
      "v_mfma_f32_16x16x32_bf16 v[120:123], a[4:7], a[88:91], v[120:123]\n\t"
      "buffer_load_dwordx4 a[192:195], v12, s[52:55], s69 offen sc1\n\t"
      "v_cvt_pk_bf16_f32 v220, v220, v221\n\t"
      "v_mfma_f32_16x16x32_bf16 v[124:127], a[12:15], a[88:91], v[124:127]\n\t"
      "v_cvt_pk_bf16_f32 v221, v222, v223\n\t"
      "v_add_f32_e32 v144, v184, v72\n\t"
      "v_mfma_f32_16x16x32_bf16 v[128:131], a[20:23], a[88:91], v[128:131]\n\t"
      "ds_read_b128 a[88:91], v34 offset:31744\n\t"
      "v_add_f32_e32 v145, v185, v73\n\t"
      "v_mfma_f32_16x16x32_bf16 v[132:135], a[4:7], a[92:95], v[132:135]\n\t"
      "ds_write_b64 v18, v[212:213] offset:12288\n\t"
      "v_mfma_f32_16x16x32_bf16 v[136:139], a[12:15], a[92:95], v[136:139]\n\t"
      "v_add_f32_e32 v146, v186, v74\n\t"
      "v_add_f32_e32 v147, v187, v75\n\t"
      "v_mfma_f32_16x16x32_bf16 v[140:143], a[20:23], a[92:95], v[140:143]\n\t"
      "buffer_load_dwordx4 a[200:203], v12, s[52:55], s69 offen offset:1024 sc1\n\t"
      "v_sub_f32_e32 v72, v184, v72\n\t"
      "v_smfmac_f32_16x16x64_bf16 v[188:191], v[8:11], a[0:7], v236\n\t"
      "ds_read_b128 a[92:95], v35 offset:31744\n\t"
      "v_sub_f32_e32 v73, v185, v73\n\t"
      "v_smfmac_f32_16x16x64_bf16 v[192:195], v[8:11], a[8:15], v236\n\t"
      "ds_write_b64 v18, v[216:217] offset:12296\n\t"
      "v_smfmac_f32_16x16x64_bf16 v[196:199], v[8:11], a[16:23], v236\n\t"
      "s_waitcnt vmcnt(11)\n\t"
      "buffer_load_dwordx4 a[208:211], v12, s[52:55], s69 offen offset:2048 sc1\n\t"
      "v_mfma_f32_16x16x32_bf16 v[76:79], a[24:27], a[240:243], v[76:79]\n\t"
      "s_add_u32 s69, s69, 0xc00\n\t"
      "ds_read2_b32 v[8:9], v4 offset0:32 offset1:34\n\t"
      "v_mfma_f32_16x16x32_bf16 v[80:83], a[32:35], a[240:243], v[80:83]\n\t"
      "ds_read2_b32 v[10:11], v4 offset0:36 offset1:38\n\t"
      "v_sub_f32_e32 v74, v186, v74\n\t"
      "v_mfma_f32_16x16x32_bf16 v[84:87], a[40:43], a[240:243], v[84:87]\n\t"
      "s_waitcnt lgkmcnt(6)\n\t"
      "ds_read_b128 a[240:243], v33 offset:32768\n\t"
      "v_mfma_f32_16x16x32_bf16 v[88:91], a[24:27], a[244:247], v[88:91]\n\t"
      "ds_write_b64 v25, v[220:221] offset:12288\n\t"
      "v_mfma_f32_16x16x32_bf16 v[92:95], a[32:35], a[244:247], v[92:95]\n\t"
      "buffer_load_dwordx4 a[196:199], v12, s[52:55], s69 offen sc1\n\t"
      "v_sub_f32_e32 v75, v187, v75\n\t"
      "v_mfma_f32_16x16x32_bf16 v[96:99], a[40:43], a[244:247], v[96:99]\n\t"
      "ds_read_b128 a[244:247], v34 offset:32768\n\t"
      "v_add_f32_e32 v184, v40, v112\n\t"
      "v_mfma_f32_16x16x32_bf16 v[100:103], a[24:27], a[248:251], v[100:103]\n\t"
      "v_add_f32_e32 v185, v41, v113\n\t"
      "v_add_f32_e32 v186, v42, v114\n\t"
      "v_mfma_f32_16x16x32_bf16 v[104:107], a[32:35], a[248:251], v[104:107]\n\t"
      "v_add_f32_e32 v187, v43, v115\n\t"
      "buffer_load_dwordx4 a[204:207], v12, s[52:55], s69 offen offset:1024 sc1\n\t"
      "v_mfma_f32_16x16x32_bf16 v[108:111], a[40:43], a[248:251], v[108:111]\n\t"
      "s_waitcnt vmcnt(11)\n\t"
      "ds_read_b128 a[248:251], v35 offset:32768\n\t"
      "v_mfma_f32_16x16x32_bf16 v[76:79], a[28:31], a[252:255], v[76:79]\n\t"
      "v_sub_f32_e32 v112, v40, v112\n\t"
      "v_sub_f32_e32 v113, v41, v113\n\t"
      "v_mfma_f32_16x16x32_bf16 v[80:83], a[36:39], a[252:255], v[80:83]\n\t"
      "v_sub_f32_e32 v114, v42, v114\n\t"
      "buffer_load_dwordx4 a[212:215], v12, s[52:55], s69 offen offset:2048 sc1\n\t"
      "v_mfma_f32_16x16x32_bf16 v[84:87], a[44:47], a[252:255], v[84:87]\n\t"
      "s_waitcnt lgkmcnt(4)\n\t"
      "s_add_u32 s69, s69, 0xc00\n\t"
      "v_sub_f32_e32 v115, v43, v115\n\t"
      "v_mfma_f32_16x16x32_bf16 v[88:91], a[28:31], a[88:91], v[88:91]\n\t"
      "ds_read_b128 a[252:255], v33 offset:33792\n\t"
      "v_add_f32_e32 v40, v44, v116\n\t"
      "v_mfma_f32_16x16x32_bf16 v[92:95], a[36:39], a[88:91], v[92:95]\n\t"
      "v_add_f32_e32 v41, v45, v117\n\t"
      "v_add_f32_e32 v42, v46, v118\n\t"
      "v_mfma_f32_16x16x32_bf16 v[96:99], a[44:47], a[88:91], v[96:99]\n\t"
      "ds_read_b128 a[88:91], v34 offset:33792\n\t"
      "v_add_f32_e32 v43, v47, v119\n\t"
      "v_mfma_f32_16x16x32_bf16 v[100:103], a[28:31], a[92:95], v[100:103]\n\t"
      "buffer_load_dwordx4 a[216:219], v12, s[52:55], s69 offen sc1\n\t"
      "v_sub_f32_e32 v116, v44, v116\n\t"
      "v_mfma_f32_16x16x32_bf16 v[104:107], a[36:39], a[92:95], v[104:107]\n\t"
      "v_sub_f32_e32 v117, v45, v117\n\t"
      "v_sub_f32_e32 v118, v46, v118\n\t"
      "v_mfma_f32_16x16x32_bf16 v[108:111], a[44:47], a[92:95], v[108:111]\n\t"
      "ds_read_b128 a[92:95], v35 offset:33792\n\t"
      "v_sub_f32_e32 v119, v47, v119\n\t"
      "v_smfmac_f32_16x16x64_bf16 v[188:191], v[0:3], a[24:31], v236\n\t"
      "v_add_f32_e32 v44, v48, v120\n\t"
      "v_add_f32_e32 v45, v49, v121\n\t"
      "v_smfmac_f32_16x16x64_bf16 v[192:195], v[0:3], a[32:39], v236\n\t"
      "buffer_load_dwordx4 a[224:227], v12, s[52:55], s69 offen offset:1024 sc1\n\t"
      "v_add_f32_e32 v46, v50, v122\n\t"
      "v_smfmac_f32_16x16x64_bf16 v[196:199], v[0:3], a[40:47], v236\n\t"
      "s_waitcnt vmcnt(11) lgkmcnt(3)\n\t"
      "ds_read2_b32 v[0:1], v4 offset0:64 offset1:66\n\t"
      "v_mfma_f32_16x16x32_bf16 v[76:79], a[48:51], a[240:243], v[76:79]\n\t"
      "ds_read2_b32 v[2:3], v4 offset0:68 offset1:70\n\t"
      "v_add_f32_e32 v47, v51, v123\n\t"
      "v_mfma_f32_16x16x32_bf16 v[80:83], a[56:59], a[240:243], v[80:83]\n\t"
      "v_sub_f32_e32 v120, v48, v120\n\t"
      "buffer_load_dwordx4 a[232:235], v12, s[52:55], s69 offen offset:2048 sc1\n\t"
      "v_mfma_f32_16x16x32_bf16 v[84:87], a[64:67], a[240:243], v[84:87]\n\t"
      "s_add_u32 s69, s69, 0xc00\n\t"
      "ds_read_b128 a[240:243], v33 offset:34816\n\t"
      "v_mfma_f32_16x16x32_bf16 v[88:91], a[48:51], a[244:247], v[88:91]\n\t"
      "v_sub_f32_e32 v121, v49, v121\n\t"
      "v_sub_f32_e32 v122, v50, v122\n\t"
      "v_mfma_f32_16x16x32_bf16 v[92:95], a[56:59], a[244:247], v[92:95]\n\t"
      "v_sub_f32_e32 v123, v51, v123\n\t"
      "v_add_f32_e32 v48, v52, v124\n\t"
      "v_mfma_f32_16x16x32_bf16 v[96:99], a[64:67], a[244:247], v[96:99]\n\t"
      "buffer_load_dwordx4 a[220:223], v12, s[52:55], s69 offen sc1\n\t"
      "v_add_f32_e32 v49, v53, v125\n\t"
      "v_mfma_f32_16x16x32_bf16 v[100:103], a[48:51], a[248:251], v[100:103]\n\t"
      "ds_read_b128 a[244:247], v34 offset:34816\n\t"
      "v_add_f32_e32 v50, v54, v126\n\t"
      "v_mfma_f32_16x16x32_bf16 v[104:107], a[56:59], a[248:251], v[104:107]\n\t"
      "v_add_f32_e32 v51, v55, v127\n\t"
      "v_sub_f32_e32 v124, v52, v124\n\t"
      "v_mfma_f32_16x16x32_bf16 v[108:111], a[64:67], a[248:251], v[108:111]\n\t"
      "ds_read_b128 a[248:251], v35 offset:34816\n\t"
      "v_sub_f32_e32 v125, v53, v125\n\t"
      "v_mfma_f32_16x16x32_bf16 v[224:227], a[156:159], a[76:79], 0\n\t"
      "v_sub_f32_e32 v126, v54, v126\n\t"
      "v_sub_f32_e32 v127, v55, v127\n\t"
      "v_mfma_f32_16x16x32_bf16 v[228:231], a[172:175], a[76:79], 0\n\t"
      "buffer_load_dwordx4 a[228:231], v12, s[52:55], s69 offen offset:1024 sc1\n\t"
      "v_add_f32_e32 v52, v56, v128\n\t"
      "v_mfma_f32_16x16x32_bf16 v[232:235], a[188:191], a[76:79], 0\n\t"
      "s_waitcnt vmcnt(11) lgkmcnt(3)\n\t"
      "v_add_f32_e32 v53, v57, v129\n\t"
      "v_add_f32_e32 v54, v58, v130\n\t"
      "v_mfma_f32_16x16x32_bf16 v[76:79], a[52:55], a[252:255], v[76:79]\n\t"
      "v_add_f32_e32 v55, v59, v131\n\t"
      "v_sub_f32_e32 v128, v56, v128\n\t"
      "v_mfma_f32_16x16x32_bf16 v[80:83], a[60:63], a[252:255], v[80:83]\n\t"
      "v_sub_f32_e32 v129, v57, v129\n\t"
      "v_sub_f32_e32 v130, v58, v130\n\t"
      "v_mfma_f32_16x16x32_bf16 v[84:87], a[68:71], a[252:255], v[84:87]\n\t"
      "buffer_load_dwordx4 a[236:239], v12, s[52:55], s69 offen offset:2048 sc1\n\t"
      "s_add_u32 s69, s69, 0xc00\n\t"
      "v_mfma_f32_16x16x32_bf16 v[88:91], a[52:55], a[88:91], v[88:91]\n\t"
      "ds_read_b128 a[252:255], v33 offset:35840\n\t"
      "v_sub_f32_e32 v131, v59, v131\n\t"
      "v_mfma_f32_16x16x32_bf16 v[92:95], a[60:63], a[88:91], v[92:95]\n\t"
      "v_add_f32_e32 v56, v60, v132\n\t"
      "v_add_f32_e32 v57, v61, v133\n\t"
      "v_mfma_f32_16x16x32_bf16 v[96:99], a[68:71], a[88:91], v[96:99]\n\t"
      "ds_read_b128 a[88:91], v34 offset:35840\n\t"
      "v_add_f32_e32 v58, v62, v134\n\t"
      "v_mfma_f32_16x16x32_bf16 v[100:103], a[52:55], a[92:95], v[100:103]\n\t"
      "v_add_f32_e32 v59, v63, v135\n\t"
      "v_sub_f32_e32 v132, v60, v132\n\t"
      "v_mfma_f32_16x16x32_bf16 v[104:107], a[60:63], a[92:95], v[104:107]\n\t"
      "buffer_load_dwordx4 a[0:3], v12, s[52:55], s69 offen sc1\n\t"
      "v_sub_f32_e32 v133, v61, v133\n\t"
      "v_mfma_f32_16x16x32_bf16 v[108:111], a[68:71], a[92:95], v[108:111]\n\t"
      "ds_read_b128 a[92:95], v35 offset:35840\n\t"
      "v_sub_f32_e32 v134, v62, v134\n\t"
      "v_smfmac_f32_16x16x64_bf16 v[188:191], v[8:11], a[48:55], v236\n\t"
      "v_sub_f32_e32 v135, v63, v135\n\t"
      "v_add_f32_e32 v60, v64, v136\n\t"
      "v_smfmac_f32_16x16x64_bf16 v[192:195], v[8:11], a[56:63], v236\n\t"
      "v_add_f32_e32 v61, v65, v137\n\t"
      "v_add_f32_e32 v62, v66, v138\n\t"
      "v_smfmac_f32_16x16x64_bf16 v[196:199], v[8:11], a[64:71], v236\n\t"
      "buffer_load_dwordx4 a[8:11], v12, s[52:55], s69 offen offset:1024 sc1\n\t"
      "buffer_load_dwordx4 a[16:19], v12, s[52:55], s69 offen offset:2048 sc1\n\t"
      "s_add_u32 s69, s69, 0xc00\n\t"
      "ds_read2_b32 v[8:9], v5 offset0:0 offset1:2\n\t"
      "ds_read2_b32 v[10:11], v5 offset0:4 offset1:6\n\t"
      "v_add_f32_e32 v63, v67, v139\n\t"
      "v_sub_f32_e32 v136, v64, v136\n\t"
      "v_sub_f32_e32 v137, v65, v137\n\t"
      "v_sub_f32_e32 v138, v66, v138\n\t"
      "v_sub_f32_e32 v139, v67, v139\n\t"
      "v_add_f32_e32 v64, v68, v140\n\t"
      "v_add_f32_e32 v65, v69, v141\n\t"
      "v_add_f32_e32 v66, v70, v142\n\t"
      "v_add_f32_e32 v67, v71, v143\n\t"
      "v_sub_f32_e32 v140, v68, v140\n\t"
      "v_sub_f32_e32 v141, v69, v141\n\t"
      "v_sub_f32_e32 v142, v70, v142\n\t"
      "s_waitcnt lgkmcnt(2)\n\t"
      "v_sub_f32_e32 v143, v71, v143\n\t"
      "v_cvt_pk_bf16_f32 v224, v224, v225\n\t"
      "s_barrier\n\t"
      "s_waitcnt vmcnt(12)\n\t"
      "v_mfma_f32_16x16x32_bf16 v[76:79], a[192:195], a[240:243], v[76:79]\n\t"
      "v_cvt_pk_bf16_f32 v228, v228, v229\n\t"
      "v_cvt_pk_bf16_f32 v232, v232, v233\n\t"
      "v_mfma_f32_16x16x32_bf16 v[80:83], a[200:203], a[240:243], v[80:83]\n\t"
      "v_cvt_pk_bf16_f32 v225, v226, v227\n\t"
      "v_cvt_pk_bf16_f32 v229, v230, v231\n\t"
      "v_mfma_f32_16x16x32_bf16 v[84:87], a[208:211], a[240:243], v[84:87]\n\t"
      "buffer_load_dwordx4 a[4:7], v12, s[52:55], s69 offen sc1\n\t"
      "v_cvt_pk_bf16_f32 v233, v234, v235\n\t"
      "v_mfma_f32_16x16x32_bf16 v[88:91], a[192:195], a[244:247], v[88:91]\n\t"
      "ds_read_b128 a[240:243], v33 offset:43008\n\t"
      "v_bfe_u32 v4, v237, 16, 2\n\t"
      "v_mfma_f32_16x16x32_bf16 v[92:95], a[200:203], a[244:247], v[92:95]\n\t"
      "ds_write_b64 v27, v[224:225]\n\t"
      "v_mfma_f32_16x16x32_bf16 v[96:99], a[208:211], a[244:247], v[96:99]\n\t"
      "v_mad_u32_u24 v4, v4, s76, v238\n\t"
      "v_mfma_f32_16x16x32_bf16 v[100:103], a[192:195], a[248:251], v[100:103]\n\t"
      "buffer_load_dwordx4 a[12:15], v12, s[52:55], s69 offen offset:1024 sc1\n\t"
      "v_mfma_f32_16x16x32_bf16 v[104:107], a[200:203], a[248:251], v[104:107]\n\t"
      "ds_read_b128 a[244:247], v34 offset:43008\n\t"
      "v_mfma_f32_16x16x32_bf16 v[108:111], a[208:211], a[248:251], v[108:111]\n\t"
      "s_waitcnt vmcnt(11)\n\t"
      "v_mfma_f32_16x16x32_bf16 v[76:79], a[196:199], a[252:255], v[76:79]\n\t"
      "ds_read_b128 a[248:251], v35 offset:43008\n\t"
      "buffer_load_dwordx4 a[20:23], v12, s[52:55], s69 offen offset:2048 sc1\n\t"
      "v_mfma_f32_16x16x32_bf16 v[80:83], a[204:207], a[252:255], v[80:83]\n\t"
      "s_add_u32 s69, s69, 0xc00\n\t"
      "v_mfma_f32_16x16x32_bf16 v[84:87], a[212:215], a[252:255], v[84:87]\n\t"
      "ds_read_b128 a[252:255], v33 offset:44032\n\t"
      "v_mfma_f32_16x16x32_bf16 v[88:91], a[196:199], a[88:91], v[88:91]\n\t"
      "ds_write_b64 v27, v[228:229] offset:8\n\t"
      "v_mfma_f32_16x16x32_bf16 v[92:95], a[204:207], a[88:91], v[92:95]\n\t"
      "buffer_load_dwordx4 a[24:27], v12, s[52:55], s69 offen sc1\n\t"
      "v_mfma_f32_16x16x32_bf16 v[96:99], a[212:215], a[88:91], v[96:99]\n\t"
      "ds_read_b128 a[88:91], v34 offset:44032\n\t"
      "v_mfma_f32_16x16x32_bf16 v[100:103], a[196:199], a[92:95], v[100:103]\n\t"
      "ds_write_b64 v30, v[232:233]\n\t"
      "v_mfma_f32_16x16x32_bf16 v[104:107], a[204:207], a[92:95], v[104:107]\n\t"
      "v_mfma_f32_16x16x32_bf16 v[108:111], a[212:215], a[92:95], v[108:111]\n\t"
      "buffer_load_dwordx4 a[32:35], v12, s[52:55], s69 offen offset:1024 sc1\n\t"
      "v_smfmac_f32_16x16x64_bf16 v[188:191], v[0:3], a[192:199], v236\n\t"
      "ds_read_b128 a[92:95], v35 offset:44032\n\t"
      "v_smfmac_f32_16x16x64_bf16 v[192:195], v[0:3], a[200:207], v236\n\t"
      "v_smfmac_f32_16x16x64_bf16 v[196:199], v[0:3], a[208:215], v236\n\t"
      "ds_read2_b32 v[0:1], v5 offset0:32 offset1:34\n\t"
      "v_mfma_f32_16x16x32_bf16 v[200:203], a[96:99], a[72:75], 0\n\t"
      "buffer_load_dwordx4 a[40:43], v12, s[52:55], s69 offen offset:2048 sc1\n\t"
      "s_add_u32 s69, s69, 0xc00\n\t"
      "v_mfma_f32_16x16x32_bf16 v[204:207], a[112:115], a[72:75], 0\n\t"
      "ds_read2_b32 v[2:3], v5 offset0:36 offset1:38\n\t"
      "v_mfma_f32_16x16x32_bf16 v[208:211], a[128:131], a[72:75], 0\n\t"
      "s_waitcnt vmcnt(12) lgkmcnt(5)\n\t"
      "v_mfma_f32_16x16x32_bf16 v[148:151], a[216:219], a[240:243], v[148:151]\n\t"
      "s_nop 0\n\t"
      "v_cvt_pk_bf16_f32 v200, v200, v201\n\t"
      "v_cvt_pk_bf16_f32 v201, v202, v203\n\t"
      "v_mfma_f32_16x16x32_bf16 v[152:155], a[224:227], a[240:243], v[152:155]\n\t"
      "v_cvt_pk_bf16_f32 v204, v204, v205\n\t"
      "v_cvt_pk_bf16_f32 v205, v206, v207\n\t"
      "v_mfma_f32_16x16x32_bf16 v[156:159], a[232:235], a[240:243], v[156:159]\n\t"
      "buffer_load_dwordx4 a[28:31], v12, s[52:55], s69 offen sc1\n\t"
      "v_cvt_pk_bf16_f32 v208, v208, v209\n\t"
      "v_mfma_f32_16x16x32_bf16 v[160:163], a[216:219], a[244:247], v[160:163]\n\t"
      "ds_read_b128 a[240:243], v33 offset:45056\n\t"
      "v_cvt_pk_bf16_f32 v209, v210, v211\n\t"
      "v_mfma_f32_16x16x32_bf16 v[164:167], a[224:227], a[244:247], v[164:167]\n\t"
      "ds_write_b64 v13, v[200:201] offset:24576\n\t"
      "v_mfma_f32_16x16x32_bf16 v[168:171], a[232:235], a[244:247], v[168:171]\n\t"
      "v_mfma_f32_16x16x32_bf16 v[172:175], a[216:219], a[248:251], v[172:175]\n\t"
      "buffer_load_dwordx4 a[36:39], v12, s[52:55], s69 offen offset:1024 sc1\n\t"
      "v_mfma_f32_16x16x32_bf16 v[176:179], a[224:227], a[248:251], v[176:179]\n\t"
      "ds_read_b128 a[244:247], v34 offset:45056\n\t"
      "v_mfma_f32_16x16x32_bf16 v[180:183], a[232:235], a[248:251], v[180:183]\n\t"
      "s_waitcnt vmcnt(11)\n\t"
      "v_mfma_f32_16x16x32_bf16 v[148:151], a[220:223], a[252:255], v[148:151]\n\t"
      "ds_read_b128 a[248:251], v35 offset:45056\n\t"
      "buffer_load_dwordx4 a[44:47], v12, s[52:55], s69 offen offset:2048 sc1\n\t"
      "v_mfma_f32_16x16x32_bf16 v[152:155], a[228:231], a[252:255], v[152:155]\n\t"
      "s_add_u32 s69, s69, 0xc00\n\t"
      "v_mfma_f32_16x16x32_bf16 v[156:159], a[236:239], a[252:255], v[156:159]\n\t"
      "s_waitcnt lgkmcnt(4)\n\t"
      "ds_read_b128 a[252:255], v33 offset:46080\n\t"
      "v_mfma_f32_16x16x32_bf16 v[160:163], a[220:223], a[88:91], v[160:163]\n\t"
      "ds_write_b64 v13, v[204:205] offset:24584\n\t"
      "v_mfma_f32_16x16x32_bf16 v[164:167], a[228:231], a[88:91], v[164:167]\n\t"
      "v_mfma_f32_16x16x32_bf16 v[168:171], a[236:239], a[88:91], v[168:171]\n\t"
      "buffer_load_dwordx4 a[48:51], v12, s[52:55], s69 offen sc1\n\t"
      "v_mfma_f32_16x16x32_bf16 v[172:175], a[220:223], a[92:95], v[172:175]\n\t"
      "ds_read_b128 a[88:91], v34 offset:46080\n\t"
      "v_mfma_f32_16x16x32_bf16 v[176:179], a[228:231], a[92:95], v[176:179]\n\t"
      "ds_write_b64 v20, v[208:209] offset:24576\n\t"
      "v_mfma_f32_16x16x32_bf16 v[180:183], a[236:239], a[92:95], v[180:183]\n\t"
      "v_smfmac_f32_16x16x64_bf16 v[188:191], v[8:11], a[216:223], v236\n\t"
      "buffer_load_dwordx4 a[56:59], v12, s[52:55], s69 offen offset:1024 sc1\n\t"
      "v_smfmac_f32_16x16x64_bf16 v[192:195], v[8:11], a[224:231], v236\n\t"
      "ds_read_b128 a[92:95], v35 offset:46080\n\t"
      "v_smfmac_f32_16x16x64_bf16 v[196:199], v[8:11], a[232:239], v236\n\t"
      "v_mfma_f32_16x16x32_bf16 v[212:215], a[98:101], a[72:75], 0\n\t"
      "ds_read2_b32 v[8:9], v5 offset0:64 offset1:66\n\t"
      "v_mfma_f32_16x16x32_bf16 v[216:219], a[114:117], a[72:75], 0\n\t"
      "buffer_load_dwordx4 a[64:67], v12, s[52:55], s69 offen offset:2048 sc1\n\t"
      "s_add_u32 s69, s69, 0xc00\n\t"
      "v_mfma_f32_16x16x32_bf16 v[220:223], a[130:133], a[72:75], 0\n\t"
      "s_waitcnt vmcnt(12) lgkmcnt(4)\n\t"
      "ds_read2_b32 v[10:11], v5 offset0:68 offset1:70\n\t"
      "v_mfma_f32_16x16x32_bf16 v[148:151], a[0:3], a[240:243], v[148:151]\n\t"
      "v_cvt_pk_bf16_f32 v212, v212, v213\n\t"
      "v_cvt_pk_bf16_f32 v213, v214, v215\n\t"
      "v_mfma_f32_16x16x32_bf16 v[152:155], a[8:11], a[240:243], v[152:155]\n\t"
      "v_cvt_pk_bf16_f32 v216, v216, v217\n\t"
      "v_cvt_pk_bf16_f32 v217, v218, v219\n\t"
      "v_mfma_f32_16x16x32_bf16 v[156:159], a[16:19], a[240:243], v[156:159]\n\t"
      "buffer_load_dwordx4 a[52:55], v12, s[52:55], s69 offen sc1\n\t"
      "v_cvt_pk_bf16_f32 v220, v220, v221\n\t"
      "v_mfma_f32_16x16x32_bf16 v[160:163], a[0:3], a[244:247], v[160:163]\n\t"
      "ds_read_b128 a[240:243], v33 offset:47104\n\t"
      "v_cvt_pk_bf16_f32 v221, v222, v223\n\t"
      "v_mfma_f32_16x16x32_bf16 v[164:167], a[8:11], a[244:247], v[164:167]\n\t"
      "ds_write_b64 v14, v[212:213] offset:24576\n\t"
      "v_mfma_f32_16x16x32_bf16 v[168:171], a[16:19], a[244:247], v[168:171]\n\t"
      "v_bfe_u32 v5, v237, 18, 2\n\t"
      "v_mfma_f32_16x16x32_bf16 v[172:175], a[0:3], a[248:251], v[172:175]\n\t"
      "buffer_load_dwordx4 a[60:63], v12, s[52:55], s69 offen offset:1024 sc1\n\t"
      "v_mad_u32_u24 v5, v5, s76, v238\n\t"
      "v_mfma_f32_16x16x32_bf16 v[176:179], a[8:11], a[248:251], v[176:179]\n\t"
      "ds_read_b128 a[244:247], v34 offset:47104\n\t"
      "v_add_u32_e32 v5, 0x180, v5\n\t"
      "v_mfma_f32_16x16x32_bf16 v[180:183], a[16:19], a[248:251], v[180:183]\n\t"
      "s_waitcnt vmcnt(11)\n\t"
      "v_mfma_f32_16x16x32_bf16 v[148:151], a[4:7], a[252:255], v[148:151]\n\t"
      "ds_read_b128 a[248:251], v35 offset:47104\n\t"
      "buffer_load_dwordx4 a[68:71], v12, s[52:55], s69 offen offset:2048 sc1\n\t"
      "v_mfma_f32_16x16x32_bf16 v[152:155], a[12:15], a[252:255], v[152:155]\n\t"
      "s_add_u32 s69, s69, 0xc00\n\t"
      "v_mfma_f32_16x16x32_bf16 v[156:159], a[20:23], a[252:255], v[156:159]\n\t"
      "s_waitcnt lgkmcnt(4)\n\t"
      "ds_read_b128 a[252:255], v33 offset:48128\n\t"
      "v_mfma_f32_16x16x32_bf16 v[160:163], a[4:7], a[88:91], v[160:163]\n\t"
      "ds_write_b64 v14, v[216:217] offset:24584\n\t"
      "v_mfma_f32_16x16x32_bf16 v[164:167], a[12:15], a[88:91], v[164:167]\n\t"
      "v_mfma_f32_16x16x32_bf16 v[168:171], a[20:23], a[88:91], v[168:171]\n\t"
      "ds_read_b128 a[88:91], v34 offset:48128\n\t"
      "v_mfma_f32_16x16x32_bf16 v[172:175], a[4:7], a[92:95], v[172:175]\n\t"
      "buffer_load_dwordx4 a[192:195], v12, s[52:55], s69 offen sc1\n\t"
      "v_mfma_f32_16x16x32_bf16 v[176:179], a[12:15], a[92:95], v[176:179]\n\t"
      "ds_write_b64 v21, v[220:221] offset:24576\n\t"
      "v_mfma_f32_16x16x32_bf16 v[180:183], a[20:23], a[92:95], v[180:183]\n\t"
      "ds_read_b128 a[92:95], v35 offset:48128\n\t"
      "v_smfmac_f32_16x16x64_bf16 v[188:191], v[0:3], a[0:7], v236\n\t"
      "v_smfmac_f32_16x16x64_bf16 v[192:195], v[0:3], a[8:15], v236\n\t"
      "buffer_load_dwordx4 a[200:203], v12, s[52:55], s69 offen offset:1024 sc1\n\t"
      "v_smfmac_f32_16x16x64_bf16 v[196:199], v[0:3], a[16:23], v236\n\t"
      "v_mfma_f32_16x16x32_bf16 v[224:227], a[100:103], a[72:75], 0\n\t"
      "v_mfma_f32_16x16x32_bf16 v[228:231], a[116:119], a[72:75], 0\n\t"
      "v_mfma_f32_16x16x32_bf16 v[232:235], a[132:135], a[72:75], 0\n\t"
      "buffer_load_dwordx4 a[208:211], v12, s[52:55], s69 offen offset:2048 sc1\n\t"
      "s_add_u32 s69, s69, 0xc00\n\t"
      "s_nop 3\n\t"
      "v_cvt_pk_bf16_f32 v224, v224, v225\n\t"
      "v_cvt_pk_bf16_f32 v228, v228, v229\n\t"
      "v_cvt_pk_bf16_f32 v225, v226, v227\n\t"
      "v_cvt_pk_bf16_f32 v229, v230, v231\n\t"
      "s_waitcnt lgkmcnt(0)\n\t"
      "v_cvt_pk_bf16_f32 v232, v232, v233\n\t"
      "v_cvt_pk_bf16_f32 v233, v234, v235\n\t"
      "s_barrier\n\t"
      "s_waitcnt vmcnt(12)\n\t"
      "v_mfma_f32_16x16x32_bf16 v[148:151], a[24:27], a[240:243], v[148:151]\n\t"
      "buffer_load_dwordx4 a[196:199], v12, s[52:55], s69 offen sc1\n\t"
      "v_mfma_f32_16x16x32_bf16 v[152:155], a[32:35], a[240:243], v[152:155]\n\t"
      "ds_read2_b32 v[0:1], v4 offset0:0 offset1:2\n\t"
      "v_mfma_f32_16x16x32_bf16 v[156:159], a[40:43], a[240:243], v[156:159]\n\t"
      "ds_read_b128 a[240:243], v33\n\t"
      "v_mfma_f32_16x16x32_bf16 v[160:163], a[24:27], a[244:247], v[160:163]\n\t"
      "ds_write_b64 v15, v[224:225] offset:24576\n\t"
      "v_mfma_f32_16x16x32_bf16 v[164:167], a[32:35], a[244:247], v[164:167]\n\t"
      "buffer_load_dwordx4 a[204:207], v12, s[52:55], s69 offen offset:1024 sc1\n\t"
      "v_mfma_f32_16x16x32_bf16 v[168:171], a[40:43], a[244:247], v[168:171]\n\t"
      "ds_read_b128 a[244:247], v34\n\t"
      "v_mfma_f32_16x16x32_bf16 v[172:175], a[24:27], a[248:251], v[172:175]\n\t"
      "ds_write_b64 v15, v[228:229] offset:24584\n\t"
      "v_mfma_f32_16x16x32_bf16 v[176:179], a[32:35], a[248:251], v[176:179]\n\t"
      "buffer_load_dwordx4 a[212:215], v12, s[52:55], s69 offen offset:2048 sc1\n\t"
      "s_add_u32 s69, s69, 0xc00\n\t"
      "v_mfma_f32_16x16x32_bf16 v[180:183], a[40:43], a[248:251], v[180:183]\n\t"
      "s_waitcnt vmcnt(12)\n\t"
      "ds_read_b128 a[248:251], v35\n\t"
      "v_mfma_f32_16x16x32_bf16 v[148:151], a[28:31], a[252:255], v[148:151]\n\t"
      "ds_write_b64 v22, v[232:233] offset:24576\n\t"
      "v_mfma_f32_16x16x32_bf16 v[152:155], a[36:39], a[252:255], v[152:155]\n\t"
      "v_mfma_f32_16x16x32_bf16 v[156:159], a[44:47], a[252:255], v[156:159]\n\t"
      "buffer_load_dwordx4 a[216:219], v12, s[52:55], s69 offen sc1\n\t"
      "v_mfma_f32_16x16x32_bf16 v[160:163], a[28:31], a[88:91], v[160:163]\n\t"
      "ds_read_b128 a[252:255], v33 offset:1024\n\t"
      "v_mfma_f32_16x16x32_bf16 v[164:167], a[36:39], a[88:91], v[164:167]\n\t"
      "s_nop 0\n\t"
      "v_add_f32_e32 v68, v76, v148\n\t"
      "v_add_f32_e32 v69, v77, v149\n\t"
      "v_mfma_f32_16x16x32_bf16 v[168:171], a[44:47], a[88:91], v[168:171]\n\t"
      "ds_read_b128 a[88:91], v34 offset:1024\n\t"
      "v_add_f32_e32 v70, v78, v150\n\t"
      "v_mfma_f32_16x16x32_bf16 v[172:175], a[28:31], a[92:95], v[172:175]\n\t"
      "v_add_f32_e32 v71, v79, v151\n\t"
      "v_sub_f32_e32 v148, v76, v148\n\t"
      "v_mfma_f32_16x16x32_bf16 v[176:179], a[36:39], a[92:95], v[176:179]\n\t"
      "buffer_load_dwordx4 a[224:227], v12, s[52:55], s69 offen offset:1024 sc1\n\t"
      "v_sub_f32_e32 v149, v77, v149\n\t"
      "v_mfma_f32_16x16x32_bf16 v[180:183], a[44:47], a[92:95], v[180:183]\n\t"
      "ds_read_b128 a[92:95], v35 offset:1024\n\t"
      "v_sub_f32_e32 v150, v78, v150\n\t"
      "v_smfmac_f32_16x16x64_bf16 v[188:191], v[8:11], a[24:31], v236\n\t"
      "v_sub_f32_e32 v151, v79, v151\n\t"
      "v_add_f32_e32 v76, v80, v152\n\t"
      "v_smfmac_f32_16x16x64_bf16 v[192:195], v[8:11], a[32:39], v236\n\t"
      "ds_read2_b32 v[2:3], v4 offset0:4 offset1:6\n\t"
      "buffer_load_dwordx4 a[232:235], v12, s[52:55], s69 offen offset:2048 sc1\n\t"
      "v_smfmac_f32_16x16x64_bf16 v[196:199], v[8:11], a[40:47], v236\n\t"
      "s_add_u32 s69, s69, 0xc00\n\t"
      "ds_read2_b32 v[8:9], v4 offset0:32 offset1:34\n\t"
      "v_mfma_f32_16x16x32_bf16 v[200:203], a[102:105], a[72:75], 0\n\t"
      "ds_read2_b32 v[10:11], v4 offset0:36 offset1:38\n\t"
      "v_add_f32_e32 v77, v81, v153\n\t"
      "v_mfma_f32_16x16x32_bf16 v[204:207], a[118:121], a[72:75], 0\n\t"
      "v_add_f32_e32 v78, v82, v154\n\t"
      "v_add_f32_e32 v79, v83, v155\n\t"
      "v_mfma_f32_16x16x32_bf16 v[208:211], a[134:137], a[72:75], 0\n\t"
      "s_waitcnt vmcnt(12) lgkmcnt(5)\n\t"
      "v_sub_f32_e32 v152, v80, v152\n\t"
      "v_sub_f32_e32 v153, v81, v153\n\t"
      "v_mfma_f32_16x16x32_bf16 v[144:147], a[48:51], a[240:243], v[144:147]\n\t"
      "v_sub_f32_e32 v154, v82, v154\n\t"
      "v_sub_f32_e32 v155, v83, v155\n\t"
      "v_mfma_f32_16x16x32_bf16 v[184:187], a[56:59], a[240:243], v[184:187]\n\t"
      "buffer_load_dwordx4 a[220:223], v12, s[52:55], s69 offen sc1\n\t"
      "v_add_f32_e32 v80, v84, v156\n\t"
      "v_mfma_f32_16x16x32_bf16 v[40:43], a[64:67], a[240:243], v[40:43]\n\t"
      "ds_read_b128 a[240:243], v33 offset:2048\n\t"
      "v_add_f32_e32 v81, v85, v157\n\t"
      "v_mfma_f32_16x16x32_bf16 v[44:47], a[48:51], a[244:247], v[44:47]\n\t"
      "v_add_f32_e32 v82, v86, v158\n\t"
      "v_add_f32_e32 v83, v87, v159\n\t"
      "v_mfma_f32_16x16x32_bf16 v[48:51], a[56:59], a[244:247], v[48:51]\n\t"
      "v_sub_f32_e32 v156, v84, v156\n\t"
      "buffer_load_dwordx4 a[228:231], v12, s[52:55], s69 offen offset:1024 sc1\n\t"
      "v_mfma_f32_16x16x32_bf16 v[52:55], a[64:67], a[244:247], v[52:55]\n\t"
      "ds_read_b128 a[244:247], v34 offset:2048\n\t"
      "v_sub_f32_e32 v157, v85, v157\n\t"
      "v_mfma_f32_16x16x32_bf16 v[56:59], a[48:51], a[248:251], v[56:59]\n\t"
      "v_sub_f32_e32 v158, v86, v158\n\t"
      "v_sub_f32_e32 v159, v87, v159\n\t"
      "v_mfma_f32_16x16x32_bf16 v[60:63], a[56:59], a[248:251], v[60:63]\n\t"
      "v_add_f32_e32 v84, v88, v160\n\t"
      "v_add_f32_e32 v85, v89, v161\n\t"
      "v_mfma_f32_16x16x32_bf16 v[64:67], a[64:67], a[248:251], v[64:67]\n\t"
      "s_waitcnt vmcnt(11)\n\t"
      "buffer_load_dwordx4 a[236:239], v12, s[52:55], s69 offen offset:2048 sc1\n\t"
      "v_mfma_f32_16x16x32_bf16 v[144:147], a[52:55], a[252:255], v[144:147]\n\t"
      "s_add_u32 s69, s69, 0xc00\n\t"
      "ds_read_b128 a[248:251], v35 offset:2048\n\t"
      "v_mfma_f32_16x16x32_bf16 v[184:187], a[60:63], a[252:255], v[184:187]\n\t"
      "v_add_f32_e32 v86, v90, v162\n\t"
      "v_add_f32_e32 v87, v91, v163\n\t"
      "v_mfma_f32_16x16x32_bf16 v[40:43], a[68:71], a[252:255], v[40:43]\n\t"
      "s_waitcnt lgkmcnt(3)\n\t"
      "ds_read_b128 a[252:255], v33 offset:3072\n\t"
      "v_mfma_f32_16x16x32_bf16 v[44:47], a[52:55], a[88:91], v[44:47]\n\t"
      "v_sub_f32_e32 v160, v88, v160\n\t"
      "v_sub_f32_e32 v161, v89, v161\n\t"
      "v_mfma_f32_16x16x32_bf16 v[48:51], a[60:63], a[88:91], v[48:51]\n\t"
      "buffer_load_dwordx4 a[0:3], v12, s[52:55], s69 offen sc1\n\t"
      "v_sub_f32_e32 v162, v90, v162\n\t"
      "v_mfma_f32_16x16x32_bf16 v[52:55], a[68:71], a[88:91], v[52:55]\n\t"
      "ds_read_b128 a[88:91], v34 offset:3072\n\t"
      "v_sub_f32_e32 v163, v91, v163\n\t"
      "v_mfma_f32_16x16x32_bf16 v[56:59], a[52:55], a[92:95], v[56:59]\n\t"
      "v_add_f32_e32 v88, v92, v164\n\t"
      "v_add_f32_e32 v89, v93, v165\n\t"
      "v_mfma_f32_16x16x32_bf16 v[60:63], a[60:63], a[92:95], v[60:63]\n\t"
      "v_add_f32_e32 v90, v94, v166\n\t"
      "buffer_load_dwordx4 a[8:11], v12, s[52:55], s69 offen offset:1024 sc1\n\t"
      "v_mfma_f32_16x16x32_bf16 v[64:67], a[68:71], a[92:95], v[64:67]\n\t"
      "ds_read_b128 a[92:95], v35 offset:3072\n\t"
      "v_add_f32_e32 v91, v95, v167\n\t"
      "v_smfmac_f32_16x16x64_bf16 v[188:191], v[0:3], a[48:55], v236\n\t"
      "v_sub_f32_e32 v164, v92, v164\n\t"
      "v_sub_f32_e32 v165, v93, v165\n\t"
      "v_smfmac_f32_16x16x64_bf16 v[192:195], v[0:3], a[56:63], v236\n\t"
      "v_sub_f32_e32 v166, v94, v166\n\t"
      "buffer_load_dwordx4 a[16:19], v12, s[52:55], s69 offen offset:2048 sc1\n\t"
      "v_smfmac_f32_16x16x64_bf16 v[196:199], v[0:3], a[64:71], v236\n\t"
      "s_waitcnt vmcnt(12) lgkmcnt(3)\n\t"
      "s_add_u32 s69, s69, 0xc00\n\t"
      "v_sub_f32_e32 v167, v95, v167\n\t"
      "v_mfma_f32_16x16x32_bf16 v[144:147], a[192:195], a[240:243], v[144:147]\n\t"
      "ds_read2_b32 v[0:1], v4 offset0:64 offset1:66\n\t"
      "v_add_f32_e32 v92, v96, v168\n\t"
      "v_mfma_f32_16x16x32_bf16 v[184:187], a[200:203], a[240:243], v[184:187]\n\t"
      "ds_read2_b32 v[2:3], v4 offset0:68 offset1:70\n\t"
      "v_add_f32_e32 v93, v97, v169\n\t"
      "v_mfma_f32_16x16x32_bf16 v[40:43], a[208:211], a[240:243], v[40:43]\n\t"
      "ds_read_b128 a[240:243], v33 offset:4096\n\t"
      "v_add_f32_e32 v94, v98, v170\n\t"
      "v_mfma_f32_16x16x32_bf16 v[44:47], a[192:195], a[244:247], v[44:47]\n\t"
      "buffer_load_dwordx4 a[4:7], v12, s[52:55], s69 offen sc1\n\t"
      "v_add_f32_e32 v95, v99, v171\n\t"
      "v_mfma_f32_16x16x32_bf16 v[48:51], a[200:203], a[244:247], v[48:51]\n\t"
      "v_sub_f32_e32 v168, v96, v168\n\t"
      "v_sub_f32_e32 v169, v97, v169\n\t"
      "v_mfma_f32_16x16x32_bf16 v[52:55], a[208:211], a[244:247], v[52:55]\n\t"
      "ds_read_b128 a[244:247], v34 offset:4096\n\t"
      "v_sub_f32_e32 v170, v98, v170\n\t"
      "v_mfma_f32_16x16x32_bf16 v[56:59], a[192:195], a[248:251], v[56:59]\n\t"
      "v_sub_f32_e32 v171, v99, v171\n\t"
      "v_add_f32_e32 v96, v100, v172\n\t"
      "v_mfma_f32_16x16x32_bf16 v[60:63], a[200:203], a[248:251], v[60:63]\n\t"
      "buffer_load_dwordx4 a[12:15], v12, s[52:55], s69 offen offset:1024 sc1\n\t"
      "v_add_f32_e32 v97, v101, v173\n\t"
      "v_mfma_f32_16x16x32_bf16 v[64:67], a[208:211], a[248:251], v[64:67]\n\t"
      "s_waitcnt vmcnt(11) lgkmcnt(4)\n\t"
      "ds_read_b128 a[248:251], v35 offset:4096\n\t"
      "v_mfma_f32_16x16x32_bf16 v[144:147], a[196:199], a[252:255], v[144:147]\n\t"
      "v_add_f32_e32 v98, v102, v174\n\t"
      "v_add_f32_e32 v99, v103, v175\n\t"
      "v_mfma_f32_16x16x32_bf16 v[184:187], a[204:207], a[252:255], v[184:187]\n\t"
      "v_sub_f32_e32 v172, v100, v172\n\t"
      "buffer_load_dwordx4 a[20:23], v12, s[52:55], s69 offen offset:2048 sc1\n\t"
      "v_mfma_f32_16x16x32_bf16 v[40:43], a[212:215], a[252:255], v[40:43]\n\t"
      "s_add_u32 s69, s69, 0xc00\n\t"
      "ds_read_b128 a[252:255], v33 offset:5120\n\t"
      "v_mfma_f32_16x16x32_bf16 v[44:47], a[196:199], a[88:91], v[44:47]\n\t"
      "v_sub_f32_e32 v173, v101, v173\n\t"
      "v_sub_f32_e32 v174, v102, v174\n\t"
      "v_mfma_f32_16x16x32_bf16 v[48:51], a[204:207], a[88:91], v[48:51]\n\t"
      "v_sub_f32_e32 v175, v103, v175\n\t"
      "v_add_f32_e32 v100, v104, v176\n\t"
      "v_mfma_f32_16x16x32_bf16 v[52:55], a[212:215], a[88:91], v[52:55]\n\t"
      "ds_read_b128 a[88:91], v34 offset:5120\n\t"
      "v_add_f32_e32 v101, v105, v177\n\t"
      "v_mfma_f32_16x16x32_bf16 v[56:59], a[196:199], a[92:95], v[56:59]\n\t"
      "v_add_f32_e32 v102, v106, v178\n\t"
      "v_add_f32_e32 v103, v107, v179\n\t"
      "v_mfma_f32_16x16x32_bf16 v[60:63], a[204:207], a[92:95], v[60:63]\n\t"
      "buffer_load_dwordx4 a[24:27], v12, s[52:55], s69 offen sc1\n\t"
      "v_cvt_pk_bf16_f32 v200, v200, v201\n\t"
      "v_mfma_f32_16x16x32_bf16 v[64:67], a[212:215], a[92:95], v[64:67]\n\t"
      "ds_read_b128 a[92:95], v35 offset:5120\n\t"
      "v_cvt_pk_bf16_f32 v204, v204, v205\n\t"
      "v_smfmac_f32_16x16x64_bf16 v[188:191], v[8:11], a[192:199], v236\n\t"
      "v_cvt_pk_bf16_f32 v208, v208, v209\n\t"
      "v_sub_f32_e32 v176, v104, v176\n\t"
      "v_smfmac_f32_16x16x64_bf16 v[192:195], v[8:11], a[200:207], v236\n\t"
      "v_sub_f32_e32 v177, v105, v177\n\t"
      "buffer_load_dwordx4 a[32:35], v12, s[52:55], s69 offen offset:1024 sc1\n\t"
      "v_smfmac_f32_16x16x64_bf16 v[196:199], v[8:11], a[208:215], v236\n\t"
      "s_waitcnt vmcnt(11) lgkmcnt(3)\n\t"
      "ds_read2_b32 v[8:9], v5 offset0:0 offset1:2\n\t"
      "v_mfma_f32_16x16x32_bf16 v[144:147], a[216:219], a[240:243], v[144:147]\n\t"
      "ds_read2_b32 v[10:11], v5 offset0:4 offset1:6\n\t"
      "v_sub_f32_e32 v178, v106, v178\n\t"
      "v_mfma_f32_16x16x32_bf16 v[184:187], a[224:227], a[240:243], v[184:187]\n\t"
      "v_sub_f32_e32 v179, v107, v179\n\t"
      "buffer_load_dwordx4 a[40:43], v12, s[52:55], s69 offen offset:2048 sc1\n\t"
      "v_mfma_f32_16x16x32_bf16 v[40:43], a[232:235], a[240:243], v[40:43]\n\t"
      "s_add_u32 s69, s69, 0xc00\n\t"
      "ds_read_b128 a[240:243], v33 offset:6144\n\t"
      "v_mfma_f32_16x16x32_bf16 v[44:47], a[216:219], a[244:247], v[44:47]\n\t"
      "v_cvt_pk_bf16_f32 v201, v202, v203\n\t"
      "v_cvt_pk_bf16_f32 v205, v206, v207\n\t"
      "v_mfma_f32_16x16x32_bf16 v[48:51], a[224:227], a[244:247], v[48:51]\n\t"
      "v_cvt_pk_bf16_f32 v209, v210, v211\n\t"
      "v_add_f32_e32 v104, v108, v180\n\t"
      "v_mfma_f32_16x16x32_bf16 v[52:55], a[232:235], a[244:247], v[52:55]\n\t"
      "ds_read_b128 a[244:247], v34 offset:6144\n\t"
      "v_add_f32_e32 v105, v109, v181\n\t"
      "v_mfma_f32_16x16x32_bf16 v[56:59], a[216:219], a[248:251], v[56:59]\n\t"
      "buffer_load_dwordx4 a[28:31], v12, s[52:55], s69 offen sc1\n\t"
      "v_add_f32_e32 v106, v110, v182\n\t"
      "v_mfma_f32_16x16x32_bf16 v[60:63], a[224:227], a[248:251], v[60:63]\n\t"
      "v_add_f32_e32 v107, v111, v183\n\t"
      "v_sub_f32_e32 v180, v108, v180\n\t"
      "v_mfma_f32_16x16x32_bf16 v[64:67], a[232:235], a[248:251], v[64:67]\n\t"
      "ds_read_b128 a[248:251], v35 offset:6144\n\t"
      "v_sub_f32_e32 v181, v109, v181\n\t"
      "v_mfma_f32_16x16x32_bf16 v[212:215], a[104:107], a[72:75], 0\n\t"
      "ds_write_b64 v16, v[200:201] offset:24576\n\t"
      "v_mfma_f32_16x16x32_bf16 v[216:219], a[120:123], a[72:75], 0\n\t"
      "buffer_load_dwordx4 a[36:39], v12, s[52:55], s69 offen offset:1024 sc1\n\t"
      "v_sub_f32_e32 v182, v110, v182\n\t"
      "v_mfma_f32_16x16x32_bf16 v[220:223], a[136:139], a[72:75], 0\n\t"
      "s_waitcnt vmcnt(11) lgkmcnt(4)\n\t"
      "v_sub_f32_e32 v183, v111, v183\n\t"
      "v_bfe_u32 v4, v237, 20, 2\n\t"
      "v_mfma_f32_16x16x32_bf16 v[144:147], a[220:223], a[252:255], v[144:147]\n\t"
      "ds_write_b64 v16, v[204:205] offset:24584\n\t"
      "v_mfma_f32_16x16x32_bf16 v[184:187], a[228:231], a[252:255], v[184:187]\n\t"
      "buffer_load_dwordx4 a[44:47], v12, s[52:55], s69 offen offset:2048 sc1\n\t"
      "s_add_u32 s69, s69, 0xc00\n\t"
      "v_mfma_f32_16x16x32_bf16 v[40:43], a[236:239], a[252:255], v[40:43]\n\t"
      "ds_read_b128 a[252:255], v33 offset:7168\n\t"
      "v_cvt_pk_bf16_f32 v212, v212, v213\n\t"
      "v_mfma_f32_16x16x32_bf16 v[44:47], a[220:223], a[88:91], v[44:47]\n\t"
      "ds_write_b64 v23, v[208:209] offset:24576\n\t"
      "v_mfma_f32_16x16x32_bf16 v[48:51], a[228:231], a[88:91], v[48:51]\n\t"
      "v_cvt_pk_bf16_f32 v216, v216, v217\n\t"
      "v_cvt_pk_bf16_f32 v220, v220, v221\n\t"
      "v_mfma_f32_16x16x32_bf16 v[52:55], a[236:239], a[88:91], v[52:55]\n\t"
      "buffer_load_dwordx4 a[48:51], v12, s[52:55], s69 offen sc1\n\t"
      "v_cvt_pk_bf16_f32 v213, v214, v215\n\t"
      "v_mfma_f32_16x16x32_bf16 v[56:59], a[220:223], a[92:95], v[56:59]\n\t"
      "ds_read_b128 a[88:91], v34 offset:7168\n\t"
      "v_cvt_pk_bf16_f32 v217, v218, v219\n\t"
      "v_mfma_f32_16x16x32_bf16 v[60:63], a[228:231], a[92:95], v[60:63]\n\t"
      "v_cvt_pk_bf16_f32 v221, v222, v223\n\t"
      "v_mad_u32_u24 v4, v4, s76, v238\n\t"
      "v_mfma_f32_16x16x32_bf16 v[64:67], a[236:239], a[92:95], v[64:67]\n\t"
      "ds_read_b128 a[92:95], v35 offset:7168\n\t"
      "v_add_u32_e32 v4, 0x300, v4\n\t"
      "v_smfmac_f32_16x16x64_bf16 v[188:191], v[0:3], a[216:223], v236\n\t"
      "buffer_load_dwordx4 a[56:59], v12, s[52:55], s69 offen offset:1024 sc1\n\t"
      "v_smfmac_f32_16x16x64_bf16 v[192:195], v[0:3], a[224:231], v236\n\t"
      "ds_write_b64 v17, v[212:213] offset:24576\n\t"
      "v_smfmac_f32_16x16x64_bf16 v[196:199], v[0:3], a[232:239], v236\n\t"
      "s_waitcnt vmcnt(11) lgkmcnt(5)\n\t"
      "v_mfma_f32_16x16x32_bf16 v[68:71], a[0:3], a[240:243], v[68:71]\n\t"
      "ds_read2_b32 v[0:1], v5 offset0:32 offset1:34\n\t"
      "buffer_load_dwordx4 a[64:67], v12, s[52:55], s69 offen offset:2048 sc1\n\t"
      "v_mfma_f32_16x16x32_bf16 v[76:79], a[8:11], a[240:243], v[76:79]\n\t"
      "s_add_u32 s69, s69, 0xc00\n\t"
      "ds_read2_b32 v[2:3], v5 offset0:36 offset1:38\n\t"
      "v_mfma_f32_16x16x32_bf16 v[80:83], a[16:19], a[240:243], v[80:83]\n\t"
      "ds_read_b128 a[240:243], v33 offset:8192\n\t"
      "v_mfma_f32_16x16x32_bf16 v[84:87], a[0:3], a[244:247], v[84:87]\n\t"
      "ds_write_b64 v17, v[216:217] offset:24584\n\t"
      "v_mfma_f32_16x16x32_bf16 v[88:91], a[8:11], a[244:247], v[88:91]\n\t"
      "s_add_u32 s59, s56, 16\n\ts_mul_i32 s60, s59, 43\n\ts_lshr_b32 s60, s60, 8\n\ts_mul_i32 s61, s60, 6\n\ts_sub_u32 s61, s59, s61\n\ts_lshl_b32 s61, s61, 10\n\ts_mul_i32 s62, s60, 0xc000\n\ts_add_u32 s62, s62, s61\n\ts_lshl_b32 s60, s60, 4\n\t"
      "v_mfma_f32_16x16x32_bf16 v[92:95], a[16:19], a[244:247], v[92:95]\n\t"
      "buffer_load_dwordx4 a[52:55], v12, s[52:55], s69 offen sc1\n\t"
      "v_add_u32_e32 v251, s62, v12\n\t"
      "v_mfma_f32_16x16x32_bf16 v[96:99], a[0:3], a[248:251], v[96:99]\n\t"
      "ds_read_b128 a[244:247], v34 offset:8192\n\t"
      "v_mfma_f32_16x16x32_bf16 v[100:103], a[8:11], a[248:251], v[100:103]\n\t"
      "ds_write_b64 v24, v[220:221] offset:24576\n\t"
      "v_mfma_f32_16x16x32_bf16 v[104:107], a[16:19], a[248:251], v[104:107]\n\t"
      "v_mfma_f32_16x16x32_bf16 v[224:227], a[106:109], a[72:75], 0\n\t"
      "buffer_load_dwordx4 a[60:63], v12, s[52:55], s69 offen offset:1024 sc1\n\t"
      "v_mfma_f32_16x16x32_bf16 v[228:231], a[122:125], a[72:75], 0\n\t"
      "ds_read_b128 a[248:251], v35 offset:8192\n\t"
      "v_mfma_f32_16x16x32_bf16 v[232:235], a[138:141], a[72:75], 0\n\t"
      "s_waitcnt vmcnt(11) lgkmcnt(4)\n\t"
      "v_mfma_f32_16x16x32_bf16 v[68:71], a[4:7], a[252:255], v[68:71]\n\t"
      "v_mfma_f32_16x16x32_bf16 v[76:79], a[12:15], a[252:255], v[76:79]\n\t"
      "buffer_load_dwordx4 a[68:71], v12, s[52:55], s69 offen offset:2048 sc1\n\t"
      "s_add_u32 s69, s69, 0xc00\n\t"
      "v_mfma_f32_16x16x32_bf16 v[80:83], a[20:23], a[252:255], v[80:83]\n\t"
      "ds_read_b128 a[252:255], v33 offset:9216\n\t"
      "v_cvt_pk_bf16_f32 v224, v224, v225\n\t"
      "v_mfma_f32_16x16x32_bf16 v[84:87], a[4:7], a[88:91], v[84:87]\n\t"
      "v_cvt_pk_bf16_f32 v225, v226, v227\n\t"
      "v_cvt_pk_bf16_f32 v228, v228, v229\n\t"
      "v_mfma_f32_16x16x32_bf16 v[88:91], a[12:15], a[88:91], v[88:91]\n\t"
      "v_cvt_pk_bf16_f32 v232, v232, v233\n\t"
      "v_cvt_pk_bf16_f32 v229, v230, v231\n\t"
      "v_mfma_f32_16x16x32_bf16 v[92:95], a[20:23], a[88:91], v[92:95]\n\t"
      "buffer_load_dwordx4 a[192:195], v12, s[52:55], s69 offen sc1\n\t"
      "v_cvt_pk_bf16_f32 v233, v234, v235\n\t"
      "v_mfma_f32_16x16x32_bf16 v[96:99], a[4:7], a[92:95], v[96:99]\n\t"
      "ds_read_b128 a[88:91], v34 offset:9216\n\t"
      "v_mfma_f32_16x16x32_bf16 v[100:103], a[12:15], a[92:95], v[100:103]\n\t"
      "ds_write_b64 v18, v[224:225] offset:24576\n\t"
      "v_mfma_f32_16x16x32_bf16 v[104:107], a[20:23], a[92:95], v[104:107]\n\t"
      "v_smfmac_f32_16x16x64_bf16 v[188:191], v[8:11], a[0:7], v236\n\t"
      "buffer_load_dwordx4 a[200:203], v12, s[52:55], s69 offen offset:1024 sc1\n\t"
      "v_smfmac_f32_16x16x64_bf16 v[192:195], v[8:11], a[8:15], v236\n\t"
      "ds_read_b128 a[92:95], v35 offset:9216\n\t"
      "v_smfmac_f32_16x16x64_bf16 v[196:199], v[8:11], a[16:23], v236\n\t"
      "s_waitcnt vmcnt(11)\n\t"
      "v_mfma_f32_16x16x32_bf16 v[68:71], a[24:27], a[240:243], v[68:71]\n\t"
      "ds_read2_b32 v[8:9], v5 offset0:64 offset1:66\n\t"
      "v_mfma_f32_16x16x32_bf16 v[76:79], a[32:35], a[240:243], v[76:79]\n\t"
      "buffer_load_dwordx4 a[208:211], v12, s[52:55], s69 offen offset:2048 sc1\n\t"
      "s_add_u32 s69, s69, 0xc00\n\t"
      "v_mfma_f32_16x16x32_bf16 v[80:83], a[40:43], a[240:243], v[80:83]\n\t"
      "s_waitcnt lgkmcnt(4)\n\t"
      "ds_read_b128 a[240:243], v33 offset:10240\n\t"
      "v_mfma_f32_16x16x32_bf16 v[84:87], a[24:27], a[244:247], v[84:87]\n\t"
      "ds_write_b64 v18, v[228:229] offset:24584\n\t"
      "v_mfma_f32_16x16x32_bf16 v[88:91], a[32:35], a[244:247], v[88:91]\n\t"
      "v_mfma_f32_16x16x32_bf16 v[92:95], a[40:43], a[244:247], v[92:95]\n\t"
      "buffer_load_dwordx4 a[196:199], v12, s[52:55], s69 offen sc1\n\t"
      "v_mfma_f32_16x16x32_bf16 v[96:99], a[24:27], a[248:251], v[96:99]\n\t"
      "ds_read_b128 a[244:247], v34 offset:10240\n\t"
      "v_mfma_f32_16x16x32_bf16 v[100:103], a[32:35], a[248:251], v[100:103]\n\t"
      "ds_write_b64 v25, v[232:233] offset:24576\n\t"
      "v_mfma_f32_16x16x32_bf16 v[104:107], a[40:43], a[248:251], v[104:107]\n\t"
      "v_mfma_f32_16x16x32_bf16 v[200:203], a[108:111], a[72:75], 0\n\t"
      "buffer_load_dwordx4 a[204:207], v12, s[52:55], s69 offen offset:1024 sc1\n\t"
      "v_mfma_f32_16x16x32_bf16 v[204:207], a[124:127], a[72:75], 0\n\t"
      "ds_read_b128 a[248:251], v35 offset:10240\n\t"
      "v_mfma_f32_16x16x32_bf16 v[208:211], a[140:143], a[72:75], 0\n\t"
      "s_waitcnt vmcnt(11)\n\t"
      "v_mfma_f32_16x16x32_bf16 v[68:71], a[28:31], a[252:255], v[68:71]\n\t"
      "ds_read2_b32 v[10:11], v5 offset0:68 offset1:70\n\t"
      "buffer_load_dwordx4 a[212:215], v12, s[52:55], s69 offen offset:2048 sc1\n\t"
      "v_mfma_f32_16x16x32_bf16 v[76:79], a[36:39], a[252:255], v[76:79]\n\t"
      "s_add_u32 s69, s69, 0xc00\n\t"
      "v_cvt_pk_bf16_f32 v200, v200, v201\n\t"
      "v_cvt_pk_bf16_f32 v204, v204, v205\n\t"
      "v_mfma_f32_16x16x32_bf16 v[80:83], a[44:47], a[252:255], v[80:83]\n\t"
      "s_waitcnt lgkmcnt(4)\n\t"
      "ds_read_b128 a[252:255], v33 offset:11264\n\t"
      "v_mfma_f32_16x16x32_bf16 v[84:87], a[28:31], a[88:91], v[84:87]\n\t"
      "v_cvt_pk_bf16_f32 v208, v208, v209\n\t"
      "v_cvt_pk_bf16_f32 v201, v202, v203\n\t"
      "v_mfma_f32_16x16x32_bf16 v[88:91], a[36:39], a[88:91], v[88:91]\n\t"
      "v_cvt_pk_bf16_f32 v205, v206, v207\n\t"
      "v_cvt_pk_bf16_f32 v209, v210, v211\n\t"
      "v_mfma_f32_16x16x32_bf16 v[92:95], a[44:47], a[88:91], v[92:95]\n\t"
      "buffer_load_dwordx4 a[216:219], v12, s[52:55], s69 offen sc1\n\t"
      "v_bfe_u32 v5, v237, 22, 2\n\t"
      "v_mfma_f32_16x16x32_bf16 v[96:99], a[28:31], a[92:95], v[96:99]\n\t"
      "ds_read_b128 a[88:91], v34 offset:11264\n\t"
      "v_mad_u32_u24 v5, v5, s76, v238\n\t"
      "v_mfma_f32_16x16x32_bf16 v[100:103], a[36:39], a[92:95], v[100:103]\n\t"
      "ds_write_b64 v28, v[200:201]\n\t"
      "v_mfma_f32_16x16x32_bf16 v[104:107], a[44:47], a[92:95], v[104:107]\n\t"
      "v_add_u32_e32 v5, 0x600, v5\n\t"
      "v_smfmac_f32_16x16x64_bf16 v[188:191], v[0:3], a[24:31], v236\n\t"
      "buffer_load_dwordx4 a[224:227], v12, s[52:55], s69 offen offset:1024 sc1\n\t"
      "v_smfmac_f32_16x16x64_bf16 v[192:195], v[0:3], a[32:39], v236\n\t"
      "ds_read_b128 a[92:95], v35 offset:11264\n\t"
      "v_smfmac_f32_16x16x64_bf16 v[196:199], v[0:3], a[40:47], v236\n\t"
      "s_waitcnt vmcnt(11)\n\t"
      "v_mfma_f32_16x16x32_bf16 v[68:71], a[48:51], a[240:243], v[68:71]\n\t"
      "ds_read2_b32 v[0:1], v4 offset0:0 offset1:2\n\t"
      "v_mfma_f32_16x16x32_bf16 v[76:79], a[56:59], a[240:243], v[76:79]\n\t"
      "buffer_load_dwordx4 a[232:235], v12, s[52:55], s69 offen offset:2048 sc1\n\t"
      "s_add_u32 s69, s69, 0xc00\n\t"
      "v_mfma_f32_16x16x32_bf16 v[80:83], a[64:67], a[240:243], v[80:83]\n\t"
      "s_waitcnt lgkmcnt(4)\n\t"
      "ds_read_b128 a[240:243], v33 offset:12288\n\t"
      "v_mfma_f32_16x16x32_bf16 v[84:87], a[48:51], a[244:247], v[84:87]\n\t"
      "ds_write_b64 v28, v[204:205] offset:8\n\t"
      "v_mfma_f32_16x16x32_bf16 v[88:91], a[56:59], a[244:247], v[88:91]\n\t"
      "v_add_u32_e32 v28, s61, v12\n\t"
      "v_mfma_f32_16x16x32_bf16 v[92:95], a[64:67], a[244:247], v[92:95]\n\t"
      "buffer_load_dwordx4 a[220:223], v12, s[52:55], s69 offen sc1\n\t"
      "v_lshrrev_b32_e32 v28, 4, v28\n\t"
      "v_mfma_f32_16x16x32_bf16 v[96:99], a[48:51], a[248:251], v[96:99]\n\t"
      "ds_read_b128 a[244:247], v34 offset:12288\n\t"
      "v_mul_u32_u24_e32 v30, 0xaab, v28\n\t"
      "v_mfma_f32_16x16x32_bf16 v[100:103], a[56:59], a[248:251], v[100:103]\n\t"
      "ds_write_b64 v31, v[208:209]\n\t"
      "v_mfma_f32_16x16x32_bf16 v[104:107], a[64:67], a[248:251], v[104:107]\n\t"
      "v_lshrrev_b32_e32 v30, 16, v30\n\t"
      "buffer_load_dwordx4 a[228:231], v12, s[52:55], s69 offen offset:1024 sc1\n\t"
      "v_mfma_f32_16x16x32_bf16 v[212:215], a[144:147], a[72:75], 0\n\t"
      "ds_read_b128 a[248:251], v35 offset:12288\n\t"
      "v_mul_u32_u24_e32 v31, 24, v30\n\t"
      "v_mfma_f32_16x16x32_bf16 v[216:219], a[160:163], a[72:75], 0\n\t"
      "v_sub_u32_e32 v28, v28, v31\n\t"
      "v_and_b32_e32 v31, 7, v30\n\t"
      "v_mfma_f32_16x16x32_bf16 v[220:223], a[176:179], a[72:75], 0\n\t"
      "s_waitcnt vmcnt(11)\n\t"
      "ds_read2_b32 v[2:3], v4 offset0:4 offset1:6\n\t"
      "v_mfma_f32_16x16x32_bf16 v[68:71], a[52:55], a[252:255], v[68:71]\n\t"
      "buffer_load_dwordx4 a[236:239], v12, s[52:55], s69 offen offset:2048 sc1\n\t"
      "s_add_u32 s69, s69, 0xc00\n\t"
      "v_mfma_f32_16x16x32_bf16 v[76:79], a[60:63], a[252:255], v[76:79]\n\t"
      "v_cvt_pk_bf16_f32 v212, v212, v213\n\t"
      "v_cvt_pk_bf16_f32 v216, v216, v217\n\t"
      "v_mfma_f32_16x16x32_bf16 v[80:83], a[68:71], a[252:255], v[80:83]\n\t"
      "s_waitcnt lgkmcnt(4)\n\t"
      "ds_read_b128 a[252:255], v33 offset:13312\n\t"
      "v_mfma_f32_16x16x32_bf16 v[84:87], a[52:55], a[88:91], v[84:87]\n\t"
      "v_cvt_pk_bf16_f32 v220, v220, v221\n\t"
      "v_cvt_pk_bf16_f32 v213, v214, v215\n\t"
      "v_mfma_f32_16x16x32_bf16 v[88:91], a[60:63], a[88:91], v[88:91]\n\t"
      "v_cvt_pk_bf16_f32 v217, v218, v219\n\t"
      "v_cvt_pk_bf16_f32 v221, v222, v223\n\t"
      "v_mfma_f32_16x16x32_bf16 v[92:95], a[68:71], a[88:91], v[92:95]\n\t"
      "buffer_load_dwordx4 a[0:3], v12, s[52:55], s69 offen sc1\n\t"
      "v_add_f32_e32 v108, v144, v68\n\t"
      "v_mfma_f32_16x16x32_bf16 v[96:99], a[52:55], a[92:95], v[96:99]\n\t"
      "ds_read_b128 a[88:91], v34 offset:13312\n\t"
      "v_add_f32_e32 v109, v145, v69\n\t"
      "v_mfma_f32_16x16x32_bf16 v[100:103], a[60:63], a[92:95], v[100:103]\n\t"
      "ds_write_b64 v13, v[212:213] offset:36864\n\t"
      "v_mfma_f32_16x16x32_bf16 v[104:107], a[68:71], a[92:95], v[104:107]\n\t"
      "v_add_f32_e32 v110, v146, v70\n\t"
      "buffer_load_dwordx4 a[8:11], v12, s[52:55], s69 offen offset:1024 sc1\n\t"
      "v_smfmac_f32_16x16x64_bf16 v[188:191], v[8:11], a[48:55], v236\n\t"
      "ds_read_b128 a[92:95], v35 offset:13312\n\t"
      "v_add_f32_e32 v111, v147, v71\n\t"
      "v_smfmac_f32_16x16x64_bf16 v[192:195], v[8:11], a[56:63], v236\n\t"
      "ds_write_b64 v13, v[216:217] offset:36872\n\t"
      "v_smfmac_f32_16x16x64_bf16 v[196:199], v[8:11], a[64:71], v236\n\t"
      "v_sub_f32_e32 v68, v144, v68\n\t"
      "buffer_load_dwordx4 a[16:19], v12, s[52:55], s69 offen offset:2048 sc1\n\t"
      "v_mfma_f32_16x16x32_bf16 v[224:227], a[146:149], a[72:75], 0\n\t"
      "s_add_u32 s69, s69, 0xc00\n\t"
      "ds_read2_b32 v[8:9], v4 offset0:32 offset1:34\n\t"
      "v_mfma_f32_16x16x32_bf16 v[228:231], a[162:165], a[72:75], 0\n\t"
      "ds_read2_b32 v[10:11], v4 offset0:36 offset1:38\n\t"
      "v_sub_f32_e32 v69, v145, v69\n\t"
      "v_mfma_f32_16x16x32_bf16 v[232:235], a[178:181], a[72:75], 0\n\t"
      "s_waitcnt vmcnt(12)\n\t"
      "v_sub_f32_e32 v70, v146, v70\n\t"
      "v_sub_f32_e32 v71, v147, v71\n\t"
      "v_mfma_f32_16x16x32_bf16 v[72:75], a[192:195], a[240:243], v[72:75]\n\t"
      "ds_write_b64 v20, v[220:221] offset:36864\n\t"
      "v_mfma_f32_16x16x32_bf16 v[112:115], a[200:203], a[240:243], v[112:115]\n\t"
      "buffer_load_dwordx4 a[4:7], v12, s[52:55], s69 offen sc1\n\t"
      "v_add_f32_e32 v144, v184, v76\n\t"
      "v_mfma_f32_16x16x32_bf16 v[116:119], a[208:211], a[240:243], v[116:119]\n\t"
      "s_waitcnt lgkmcnt(5)\n\t"
      "ds_read_b128 a[240:243], v33 offset:14336\n\t"
      "v_mfma_f32_16x16x32_bf16 v[120:123], a[192:195], a[244:247], v[120:123]\n\t"
      "v_add_f32_e32 v145, v185, v77\n\t"
      "v_add_f32_e32 v146, v186, v78\n\t"
      "v_mfma_f32_16x16x32_bf16 v[124:127], a[200:203], a[244:247], v[124:127]\n\t"
      "v_add_f32_e32 v147, v187, v79\n\t"
      "v_sub_f32_e32 v76, v184, v76\n\t"
      "v_mfma_f32_16x16x32_bf16 v[128:131], a[208:211], a[244:247], v[128:131]\n\t"
      "buffer_load_dwordx4 a[12:15], v12, s[52:55], s69 offen offset:1024 sc1\n\t"
      "v_sub_f32_e32 v77, v185, v77\n\t"
      "v_mfma_f32_16x16x32_bf16 v[132:135], a[192:195], a[248:251], v[132:135]\n\t"
      "ds_read_b128 a[244:247], v34 offset:14336\n\t"
      "v_sub_f32_e32 v78, v186, v78\n\t"
      "v_mfma_f32_16x16x32_bf16 v[136:139], a[200:203], a[248:251], v[136:139]\n\t"
      "v_sub_f32_e32 v79, v187, v79\n\t"
      "v_add_f32_e32 v184, v40, v80\n\t"
      "v_mfma_f32_16x16x32_bf16 v[140:143], a[208:211], a[248:251], v[140:143]\n\t"
      "s_waitcnt vmcnt(11)\n\t"
      "buffer_load_dwordx4 a[20:23], v12, s[52:55], s69 offen offset:2048 sc1\n\t"
      "v_mfma_f32_16x16x32_bf16 v[72:75], a[196:199], a[252:255], v[72:75]\n\t"
      "s_add_u32 s69, s69, 0xc00\n\t"
      "ds_read_b128 a[248:251], v35 offset:14336\n\t"
      "v_mfma_f32_16x16x32_bf16 v[112:115], a[204:207], a[252:255], v[112:115]\n\t"
      "v_add_f32_e32 v185, v41, v81\n\t"
      "v_add_f32_e32 v186, v42, v82\n\t"
      "v_mfma_f32_16x16x32_bf16 v[116:119], a[212:215], a[252:255], v[116:119]\n\t"
      "ds_read_b128 a[252:255], v33 offset:15360\n\t"
      "v_add_f32_e32 v187, v43, v83\n\t"
      "v_mfma_f32_16x16x32_bf16 v[120:123], a[196:199], a[88:91], v[120:123]\n\t"
      "v_sub_f32_e32 v80, v40, v80\n\t"
      "v_sub_f32_e32 v81, v41, v81\n\t"
      "v_mfma_f32_16x16x32_bf16 v[124:127], a[204:207], a[88:91], v[124:127]\n\t"
      "buffer_load_dwordx4 a[24:27], v12, s[52:55], s69 offen sc1\n\t"
      "v_sub_f32_e32 v82, v42, v82\n\t"
      "v_mfma_f32_16x16x32_bf16 v[128:131], a[212:215], a[88:91], v[128:131]\n\t"
      "s_waitcnt lgkmcnt(3)\n\t"
      "ds_read_b128 a[88:91], v34 offset:15360\n\t"
      "v_mfma_f32_16x16x32_bf16 v[132:135], a[196:199], a[92:95], v[132:135]\n\t"
      "v_sub_f32_e32 v83, v43, v83\n\t"
      "v_add_f32_e32 v40, v44, v84\n\t"
      "v_mfma_f32_16x16x32_bf16 v[136:139], a[204:207], a[92:95], v[136:139]\n\t"
      "v_add_f32_e32 v41, v45, v85\n\t"
      "v_add_f32_e32 v42, v46, v86\n\t"
      "v_mfma_f32_16x16x32_bf16 v[140:143], a[212:215], a[92:95], v[140:143]\n\t"
      "buffer_load_dwordx4 a[32:35], v12, s[52:55], s69 offen offset:1024 sc1\n\t"
      "v_add_f32_e32 v43, v47, v87\n\t"
      "v_smfmac_f32_16x16x64_bf16 v[188:191], v[0:3], a[192:199], v236\n\t"
      "ds_read_b128 a[92:95], v35 offset:15360\n\t"
      "v_sub_f32_e32 v84, v44, v84\n\t"
      "v_smfmac_f32_16x16x64_bf16 v[192:195], v[0:3], a[200:207], v236\n\t"
      "v_sub_f32_e32 v85, v45, v85\n\t"
      "v_sub_f32_e32 v86, v46, v86\n\t"
      "v_smfmac_f32_16x16x64_bf16 v[196:199], v[0:3], a[208:215], v236\n\t"
      "s_waitcnt vmcnt(11)\n\t"
      "ds_read2_b32 v[0:1], v4 offset0:64 offset1:66\n\t"
      "v_mfma_f32_16x16x32_bf16 v[72:75], a[216:219], a[240:243], v[72:75]\n\t"
      "buffer_load_dwordx4 a[40:43], v12, s[52:55], s69 offen offset:2048 sc1\n\t"
      "s_add_u32 s69, s69, 0xc00\n\t"
      "v_mfma_f32_16x16x32_bf16 v[112:115], a[224:227], a[240:243], v[112:115]\n\t"
      "ds_read2_b32 v[2:3], v4 offset0:68 offset1:70\n\t"
      "v_sub_f32_e32 v87, v47, v87\n\t"
      "v_mfma_f32_16x16x32_bf16 v[116:119], a[232:235], a[240:243], v[116:119]\n\t"
      "s_waitcnt lgkmcnt(4)\n\t"
      "ds_read_b128 a[240:243], v33 offset:16384\n\t"
      "v_mfma_f32_16x16x32_bf16 v[120:123], a[216:219], a[244:247], v[120:123]\n\t"
      "v_add_f32_e32 v44, v48, v88\n\t"
      "v_add_f32_e32 v45, v49, v89\n\t"
      "v_mfma_f32_16x16x32_bf16 v[124:127], a[224:227], a[244:247], v[124:127]\n\t"
      "v_add_f32_e32 v46, v50, v90\n\t"
      "v_add_f32_e32 v47, v51, v91\n\t"
      "v_mfma_f32_16x16x32_bf16 v[128:131], a[232:235], a[244:247], v[128:131]\n\t"
      "buffer_load_dwordx4 a[28:31], v12, s[52:55], s69 offen sc1\n\t"
      "v_sub_f32_e32 v88, v48, v88\n\t"
      "v_mfma_f32_16x16x32_bf16 v[132:135], a[216:219], a[248:251], v[132:135]\n\t"
      "ds_read_b128 a[244:247], v34 offset:16384\n\t"
      "v_sub_f32_e32 v89, v49, v89\n\t"
      "v_mfma_f32_16x16x32_bf16 v[136:139], a[224:227], a[248:251], v[136:139]\n\t"
      "v_sub_f32_e32 v90, v50, v90\n\t"
      "v_sub_f32_e32 v91, v51, v91\n\t"
      "v_mfma_f32_16x16x32_bf16 v[140:143], a[232:235], a[248:251], v[140:143]\n\t"
      "s_waitcnt vmcnt(10)\n\t"
      "buffer_load_dwordx4 a[36:39], v12, s[52:55], s69 offen offset:1024 sc1\n\t"
      "v_mfma_f32_16x16x32_bf16 v[72:75], a[220:223], a[252:255], v[72:75]\n\t"
      "ds_read_b128 a[248:251], v35 offset:16384\n\t"
      "v_add_f32_e32 v48, v52, v92\n\t"
      "v_mfma_f32_16x16x32_bf16 v[112:115], a[228:231], a[252:255], v[112:115]\n\t"
      "v_add_f32_e32 v49, v53, v93\n\t"
      "v_add_f32_e32 v50, v54, v94\n\t"
      "v_mfma_f32_16x16x32_bf16 v[116:119], a[236:239], a[252:255], v[116:119]\n\t"
      "s_waitcnt lgkmcnt(4)\n\t"
      "ds_read_b128 a[252:255], v33 offset:17408\n\t"
      "v_mfma_f32_16x16x32_bf16 v[120:123], a[220:223], a[88:91], v[120:123]\n\t"
      "v_add_f32_e32 v51, v55, v95\n\t"
      "buffer_load_dwordx4 a[44:47], v12, s[52:55], s69 offen offset:2048 sc1\n\t"
      "v_mfma_f32_16x16x32_bf16 v[124:127], a[228:231], a[88:91], v[124:127]\n\t"
      "s_add_u32 s69, s69, 0xc00\n\t"
      "v_sub_f32_e32 v92, v52, v92\n\t"
      "v_sub_f32_e32 v93, v53, v93\n\t"
      "v_mfma_f32_16x16x32_bf16 v[128:131], a[236:239], a[88:91], v[128:131]\n\t"
      "ds_read_b128 a[88:91], v34 offset:17408\n\t"
      "v_sub_f32_e32 v94, v54, v94\n\t"
      "v_mfma_f32_16x16x32_bf16 v[132:135], a[220:223], a[92:95], v[132:135]\n\t"
      "v_sub_f32_e32 v95, v55, v95\n\t"
      "v_add_f32_e32 v52, v56, v96\n\t"
      "v_mfma_f32_16x16x32_bf16 v[136:139], a[228:231], a[92:95], v[136:139]\n\t"
      "v_add_f32_e32 v53, v57, v97\n\t"
      "v_add_f32_e32 v54, v58, v98\n\t"
      "v_mfma_f32_16x16x32_bf16 v[140:143], a[236:239], a[92:95], v[140:143]\n\t"
      "ds_read_b128 a[92:95], v35 offset:17408\n\t"
      "v_add_f32_e32 v55, v59, v99\n\t"
      "v_smfmac_f32_16x16x64_bf16 v[188:191], v[8:11], a[216:223], v236\n\t"
      "buffer_load_dwordx4 a[48:51], v12, s[52:55], s69 offen sc1\n\t"
      "v_sub_f32_e32 v96, v56, v96\n\t"
      "v_smfmac_f32_16x16x64_bf16 v[192:195], v[8:11], a[224:231], v236\n\t"
      "v_sub_f32_e32 v97, v57, v97\n\t"
      "v_sub_f32_e32 v98, v58, v98\n\t"
      "v_smfmac_f32_16x16x64_bf16 v[196:199], v[8:11], a[232:239], v236\n\t"
      "buffer_load_dwordx4 a[56:59], v12, s[52:55], s69 offen offset:1024 sc1\n\t"
      "v_sub_f32_e32 v99, v59, v99\n\t"
      "v_add_f32_e32 v56, v60, v100\n\t"
      "v_add_f32_e32 v57, v61, v101\n\t"
      "v_add_f32_e32 v58, v62, v102\n\t"
      "v_add_f32_e32 v59, v63, v103\n\t"
      "v_cvt_pk_bf16_f32 v224, v224, v225\n\t"
      "v_cvt_pk_bf16_f32 v228, v228, v229\n\t"
      "v_cvt_pk_bf16_f32 v232, v232, v233\n\t"
      "v_sub_f32_e32 v100, v60, v100\n\t"
      "v_sub_f32_e32 v101, v61, v101\n\t"
      "v_sub_f32_e32 v102, v62, v102\n\t"
      "v_sub_f32_e32 v103, v63, v103\n\t"
      "v_cvt_pk_bf16_f32 v225, v226, v227\n\t"
      "v_cvt_pk_bf16_f32 v229, v230, v231\n\t"
      "v_cvt_pk_bf16_f32 v233, v234, v235\n\t"
      "v_add_f32_e32 v60, v64, v104\n\t"
      "v_add_f32_e32 v61, v65, v105\n\t"
      "v_add_f32_e32 v62, v66, v106\n\t"
      "s_waitcnt lgkmcnt(0)\n\t"
      "v_add_f32_e32 v63, v67, v107\n\t"
      "v_sub_f32_e32 v104, v64, v104\n\t"
      "s_barrier\n\t"
      "s_waitcnt vmcnt(11)\n\t"
      "v_mfma_f32_16x16x32_bf16 v[72:75], a[0:3], a[240:243], v[72:75]\n\t"
      "buffer_load_dwordx4 a[64:67], v12, s[52:55], s69 offen offset:2048 sc1\n\t"
      "s_add_u32 s69, s69, 0xc00\n\t"
      "v_mfma_f32_16x16x32_bf16 v[112:115], a[8:11], a[240:243], v[112:115]\n\t"
      "ds_read2_b32 v[8:9], v5 offset0:0 offset1:2\n\t"
      "v_sub_f32_e32 v105, v65, v105\n\t"
      "v_mfma_f32_16x16x32_bf16 v[116:119], a[16:19], a[240:243], v[116:119]\n\t"
      "ds_read_b128 a[240:243], v33 offset:24576\n\t"
      "v_sub_f32_e32 v106, v66, v106\n\t"
      "v_mfma_f32_16x16x32_bf16 v[120:123], a[0:3], a[244:247], v[120:123]\n\t"
      "ds_write_b64 v14, v[224:225] offset:36864\n\t"
      "v_mfma_f32_16x16x32_bf16 v[124:127], a[8:11], a[244:247], v[124:127]\n\t"
      "buffer_load_dwordx4 a[52:55], v12, s[52:55], s69 offen sc1\n\t"
      "v_sub_f32_e32 v107, v67, v107\n\t"
      "v_mfma_f32_16x16x32_bf16 v[128:131], a[16:19], a[244:247], v[128:131]\n\t"
      "ds_read_b128 a[244:247], v34 offset:24576\n\t"
      "v_add_u32_e32 v30, s60, v30\n\t"
      "v_mfma_f32_16x16x32_bf16 v[132:135], a[0:3], a[248:251], v[132:135]\n\t"
      "ds_write_b64 v14, v[228:229] offset:36872\n\t"
      "v_mfma_f32_16x16x32_bf16 v[136:139], a[8:11], a[248:251], v[136:139]\n\t"
      "v_xor_b32_e32 v28, v28, v31\n\t"
      "v_mul_u32_u24_e32 v31, 37, v30\n\t"
      "v_mfma_f32_16x16x32_bf16 v[140:143], a[16:19], a[248:251], v[140:143]\n\t"
      "buffer_load_dwordx4 a[60:63], v12, s[52:55], s69 offen offset:1024 sc1\n\t"
      "v_lshrrev_b32_e32 v31, 8, v31\n\t"
      "v_mfma_f32_16x16x32_bf16 v[200:203], a[148:151], a[72:75], 0\n\t"
      "ds_read_b128 a[248:251], v35 offset:24576\n\t"
      "v_mul_u32_u24_e32 v19, 7, v31\n\t"
      "v_mfma_f32_16x16x32_bf16 v[204:207], a[164:167], a[72:75], 0\n\t"
      "ds_write_b64 v21, v[232:233] offset:36864\n\t"
      "v_mfma_f32_16x16x32_bf16 v[208:211], a[180:183], a[72:75], 0\n\t"
      "s_waitcnt vmcnt(11)\n\t"
      "buffer_load_dwordx4 a[68:71], v12, s[52:55], s69 offen offset:2048 sc1\n\t"
      "v_mfma_f32_16x16x32_bf16 v[72:75], a[4:7], a[252:255], v[72:75]\n\t"
      "s_add_u32 s69, s69, 0xc00\n\t"
      "ds_read2_b32 v[10:11], v5 offset0:4 offset1:6\n\t"
      "v_mfma_f32_16x16x32_bf16 v[112:115], a[12:15], a[252:255], v[112:115]\n\t"
      "v_sub_u32_e32 v19, v30, v19\n\t"
      "v_mul_u32_u24_e32 v31, 0x2a000, v31\n\t"
      "v_mfma_f32_16x16x32_bf16 v[116:119], a[20:23], a[252:255], v[116:119]\n\t"
      "ds_read_b128 a[252:255], v33 offset:25600\n\t"
      "v_mul_u32_u24_e32 v19, 0x300, v19\n\t"
      "v_mfma_f32_16x16x32_bf16 v[120:123], a[4:7], a[88:91], v[120:123]\n\t"
      "buffer_load_dwordx4 a[192:195], v12, s[52:55], s69 offen sc1\n\t"
      "v_lshl_add_u32 v28, v28, 4, v31\n\t"
      "v_mfma_f32_16x16x32_bf16 v[124:127], a[12:15], a[88:91], v[124:127]\n\t"
      "v_cmp_gt_u32_e32 vcc, 48, v30\n\t"
      "v_add_u32_e32 v28, v28, v19\n\t"
      "v_mfma_f32_16x16x32_bf16 v[128:131], a[20:23], a[88:91], v[128:131]\n\t"
      "ds_read_b128 a[88:91], v34 offset:25600\n\t"
      "v_cndmask_b32_e32 v27, v39, v28, vcc\n\t"
      "v_mfma_f32_16x16x32_bf16 v[132:135], a[4:7], a[92:95], v[132:135]\n\t"
      "v_cvt_pk_bf16_f32 v200, v200, v201\n\t"
      "v_cvt_pk_bf16_f32 v204, v204, v205\n\t"
      "v_mfma_f32_16x16x32_bf16 v[136:139], a[12:15], a[92:95], v[136:139]\n\t"
      "buffer_load_dwordx4 a[200:203], v12, s[52:55], s69 offen offset:1024 sc1\n\t"
      "v_cvt_pk_bf16_f32 v208, v208, v209\n\t"
      "v_mfma_f32_16x16x32_bf16 v[140:143], a[20:23], a[92:95], v[140:143]\n\t"
      "ds_read_b128 a[92:95], v35 offset:25600\n\t"
      "v_cvt_pk_bf16_f32 v201, v202, v203\n\t"
      "v_smfmac_f32_16x16x64_bf16 v[188:191], v[0:3], a[0:7], v236\n\t"
      "v_cvt_pk_bf16_f32 v205, v206, v207\n\t"
      "v_cvt_pk_bf16_f32 v209, v210, v211\n\t"
      "v_smfmac_f32_16x16x64_bf16 v[192:195], v[0:3], a[8:15], v236\n\t"
      "ds_write_b64 v15, v[200:201] offset:36864\n\t"
      "v_smfmac_f32_16x16x64_bf16 v[196:199], v[0:3], a[16:23], v236\n\t"
      "s_waitcnt vmcnt(11) lgkmcnt(4)\n\t"
      "buffer_load_dwordx4 a[208:211], v12, s[52:55], s69 offen offset:2048 sc1\n\t"
      "v_mfma_f32_16x16x32_bf16 v[108:111], a[24:27], a[240:243], v[108:111]\n\t"
      "s_add_u32 s69, s69, 0xc00\n\t"
      "ds_read2_b32 v[0:1], v5 offset0:32 offset1:34\n\t"
      "v_mfma_f32_16x16x32_bf16 v[144:147], a[32:35], a[240:243], v[144:147]\n\t"
      "ds_read2_b32 v[2:3], v5 offset0:36 offset1:38\n\t"
      "v_bfe_u32 v4, v237, 24, 2\n\t"
      "v_mfma_f32_16x16x32_bf16 v[184:187], a[40:43], a[240:243], v[184:187]\n\t"
      "ds_read_b128 a[240:243], v33 offset:26624\n\t"
      "v_mad_u32_u24 v4, v4, s76, v238\n\t"
      "v_mfma_f32_16x16x32_bf16 v[40:43], a[24:27], a[244:247], v[40:43]\n\t"
      "ds_write_b64 v15, v[204:205] offset:36872\n\t"
      "v_mfma_f32_16x16x32_bf16 v[44:47], a[32:35], a[244:247], v[44:47]\n\t"
      "buffer_load_dwordx4 a[196:199], v12, s[52:55], s69 offen sc1\n\t"
      "v_add_u32_e32 v4, 0x480, v4\n\t"
      "v_mfma_f32_16x16x32_bf16 v[48:51], a[40:43], a[244:247], v[48:51]\n\t"
      "ds_read_b128 a[244:247], v34 offset:26624\n\t"
      "v_mfma_f32_16x16x32_bf16 v[52:55], a[24:27], a[248:251], v[52:55]\n\t"
      "ds_write_b64 v22, v[208:209] offset:36864\n\t"
      "v_mfma_f32_16x16x32_bf16 v[56:59], a[32:35], a[248:251], v[56:59]\n\t"
      "buffer_load_dwordx4 a[204:207], v12, s[52:55], s69 offen offset:1024 sc1\n\t"
      "v_mfma_f32_16x16x32_bf16 v[60:63], a[40:43], a[248:251], v[60:63]\n\t"
      "ds_read_b128 a[248:251], v35 offset:26624\n\t"
      "v_mfma_f32_16x16x32_bf16 v[212:215], a[150:153], a[72:75], 0\n\t"
      "v_mfma_f32_16x16x32_bf16 v[216:219], a[166:169], a[72:75], 0\n\t"
      "v_mfma_f32_16x16x32_bf16 v[220:223], a[182:185], a[72:75], 0\n\t"
      "s_waitcnt vmcnt(11) lgkmcnt(6)\n\t"
      "v_mfma_f32_16x16x32_bf16 v[108:111], a[28:31], a[252:255], v[108:111]\n\t"
      "buffer_load_dwordx4 a[212:215], v12, s[52:55], s69 offen offset:2048 sc1\n\t"
      "s_add_u32 s69, s69, 0xc00\n\t"
      "v_mfma_f32_16x16x32_bf16 v[144:147], a[36:39], a[252:255], v[144:147]\n\t"
      "s_nop 0\n\t"
      "v_cvt_pk_bf16_f32 v212, v212, v213\n\t"
      "v_cvt_pk_bf16_f32 v213, v214, v215\n\t"
      "v_mfma_f32_16x16x32_bf16 v[184:187], a[44:47], a[252:255], v[184:187]\n\t"
      "ds_read_b128 a[252:255], v33 offset:27648\n\t"
      "v_cvt_pk_bf16_f32 v216, v216, v217\n\t"
      "v_mfma_f32_16x16x32_bf16 v[40:43], a[28:31], a[88:91], v[40:43]\n\t"
      "v_cvt_pk_bf16_f32 v220, v220, v221\n\t"
      "v_cvt_pk_bf16_f32 v217, v218, v219\n\t"
      "v_mfma_f32_16x16x32_bf16 v[44:47], a[36:39], a[88:91], v[44:47]\n\t"
      "buffer_load_dwordx4 a[216:219], v12, s[52:55], s69 offen sc1\n\t"
      "v_cvt_pk_bf16_f32 v221, v222, v223\n\t"
      "v_mfma_f32_16x16x32_bf16 v[48:51], a[44:47], a[88:91], v[48:51]\n\t"
      "ds_read_b128 a[88:91], v34 offset:27648\n\t"
      "v_mfma_f32_16x16x32_bf16 v[52:55], a[28:31], a[92:95], v[52:55]\n\t"
      "ds_write_b64 v16, v[212:213] offset:36864\n\t"
      "v_mfma_f32_16x16x32_bf16 v[56:59], a[36:39], a[92:95], v[56:59]\n\t"
      "v_mfma_f32_16x16x32_bf16 v[60:63], a[44:47], a[92:95], v[60:63]\n\t"
      "buffer_load_dwordx4 a[224:227], v12, s[52:55], s69 offen offset:1024 sc1\n\t"
      "v_smfmac_f32_16x16x64_bf16 v[188:191], v[8:11], a[24:31], v236\n\t"
      "ds_read_b128 a[92:95], v35 offset:27648\n\t"
      "v_smfmac_f32_16x16x64_bf16 v[192:195], v[8:11], a[32:39], v236\n\t"
      "ds_write_b64 v16, v[216:217] offset:36872\n\t"
      "v_smfmac_f32_16x16x64_bf16 v[196:199], v[8:11], a[40:47], v236\n\t"
      "s_waitcnt vmcnt(11) lgkmcnt(5)\n\t"
      "v_mfma_f32_16x16x32_bf16 v[108:111], a[48:51], a[240:243], v[108:111]\n\t"
      "buffer_load_dwordx4 a[232:235], v12, s[52:55], s69 offen offset:2048 sc1\n\t"
      "s_add_u32 s69, s69, 0xc00\n\t"
      "v_mfma_f32_16x16x32_bf16 v[144:147], a[56:59], a[240:243], v[144:147]\n\t"
      "ds_read2_b32 v[8:9], v5 offset0:64 offset1:66\n\t"
      "v_mfma_f32_16x16x32_bf16 v[184:187], a[64:67], a[240:243], v[184:187]\n\t"
      "ds_read_b128 a[240:243], v33 offset:28672\n\t"
      "v_mfma_f32_16x16x32_bf16 v[40:43], a[48:51], a[244:247], v[40:43]\n\t"
      "ds_write_b64 v23, v[220:221] offset:36864\n\t"
      "v_mfma_f32_16x16x32_bf16 v[44:47], a[56:59], a[244:247], v[44:47]\n\t"
      "buffer_load_dwordx4 a[220:223], v12, s[52:55], s69 offen sc1\n\t"
      "v_mfma_f32_16x16x32_bf16 v[48:51], a[64:67], a[244:247], v[48:51]\n\t"
      "ds_read_b128 a[244:247], v34 offset:28672\n\t"
      "v_mfma_f32_16x16x32_bf16 v[52:55], a[48:51], a[248:251], v[52:55]\n\t"
      "v_mfma_f32_16x16x32_bf16 v[56:59], a[56:59], a[248:251], v[56:59]\n\t"
      "ds_read2_b32 v[10:11], v5 offset0:68 offset1:70\n\t"
      "buffer_load_dwordx4 a[228:231], v12, s[52:55], s69 offen offset:1024 sc1\n\t"
      "v_mfma_f32_16x16x32_bf16 v[60:63], a[64:67], a[248:251], v[60:63]\n\t"
      "ds_read_b128 a[248:251], v35 offset:28672\n\t"
      "v_bfe_u32 v5, v237, 26, 2\n\t"
      "v_mfma_f32_16x16x32_bf16 v[224:227], a[152:155], a[72:75], 0\n\t"
      "v_mad_u32_u24 v5, v5, s76, v238\n\t"
      "v_mfma_f32_16x16x32_bf16 v[228:231], a[168:171], a[72:75], 0\n\t"
      "v_add_u32_e32 v5, 0x780, v5\n\t"
      "v_mfma_f32_16x16x32_bf16 v[232:235], a[184:187], a[72:75], 0\n\t"
      "s_waitcnt vmcnt(11) lgkmcnt(6)\n\t"
      "buffer_load_dwordx4 a[236:239], v12, s[52:55], s69 offen offset:2048 sc1\n\t"
      "v_mfma_f32_16x16x32_bf16 v[108:111], a[52:55], a[252:255], v[108:111]\n\t"
      "s_add_u32 s69, s69, 0xc00\n\t"
      "v_cvt_pk_bf16_f32 v224, v224, v225\n\t"
      "v_cvt_pk_bf16_f32 v225, v226, v227\n\t"
      "v_mfma_f32_16x16x32_bf16 v[144:147], a[60:63], a[252:255], v[144:147]\n\t"
      "v_cvt_pk_bf16_f32 v228, v228, v229\n\t"
      "v_cvt_pk_bf16_f32 v232, v232, v233\n\t"
      "v_mfma_f32_16x16x32_bf16 v[184:187], a[68:71], a[252:255], v[184:187]\n\t"
      "ds_read_b128 a[252:255], v33 offset:29696\n\t"
      "v_cvt_pk_bf16_f32 v229, v230, v231\n\t"
      "v_mfma_f32_16x16x32_bf16 v[40:43], a[52:55], a[88:91], v[40:43]\n\t"
      "v_cvt_pk_bf16_f32 v233, v234, v235\n\t"
      "v_mfma_f32_16x16x32_bf16 v[44:47], a[60:63], a[88:91], v[44:47]\n\t"
      "buffer_load_dwordx4 a[0:3], v12, s[52:55], s69 offen sc1\n\t"
      "v_mfma_f32_16x16x32_bf16 v[48:51], a[68:71], a[88:91], v[48:51]\n\t"
      "ds_read_b128 a[88:91], v34 offset:29696\n\t"
      "v_mfma_f32_16x16x32_bf16 v[52:55], a[52:55], a[92:95], v[52:55]\n\t"
      "ds_write_b64 v17, v[224:225] offset:36864\n\t"
      "v_mfma_f32_16x16x32_bf16 v[56:59], a[60:63], a[92:95], v[56:59]\n\t"
      "v_mfma_f32_16x16x32_bf16 v[60:63], a[68:71], a[92:95], v[60:63]\n\t"
      "buffer_load_dwordx4 a[8:11], v12, s[52:55], s69 offen offset:1024 sc1\n\t"
      "v_smfmac_f32_16x16x64_bf16 v[188:191], v[0:3], a[48:55], v236\n\t"
      "ds_read_b128 a[92:95], v35 offset:29696\n\t"
      "v_smfmac_f32_16x16x64_bf16 v[192:195], v[0:3], a[56:63], v236\n\t"
      "ds_write_b64 v17, v[228:229] offset:36872\n\t"
      "v_smfmac_f32_16x16x64_bf16 v[196:199], v[0:3], a[64:71], v236\n\t"
      "s_waitcnt vmcnt(11) lgkmcnt(5)\n\t"
      "v_mfma_f32_16x16x32_bf16 v[108:111], a[192:195], a[240:243], v[108:111]\n\t"
      "buffer_load_dwordx4 a[16:19], v12, s[52:55], s69 offen offset:2048 sc1\n\t"
      "s_add_u32 s69, s69, 0xc00\n\t"
      "v_mfma_f32_16x16x32_bf16 v[144:147], a[200:203], a[240:243], v[144:147]\n\t"
      "ds_read2_b32 v[0:1], v4 offset0:0 offset1:2\n\t"
      "v_mfma_f32_16x16x32_bf16 v[184:187], a[208:211], a[240:243], v[184:187]\n\t"
      "ds_read_b128 a[240:243], v33 offset:18432\n\t"
      "v_mfma_f32_16x16x32_bf16 v[40:43], a[192:195], a[244:247], v[40:43]\n\t"
      "ds_write_b64 v24, v[232:233] offset:36864\n\t"
      "v_mfma_f32_16x16x32_bf16 v[44:47], a[200:203], a[244:247], v[44:47]\n\t"
      "buffer_load_dwordx4 a[4:7], v12, s[52:55], s69 offen sc1\n\t"
      "v_mfma_f32_16x16x32_bf16 v[48:51], a[208:211], a[244:247], v[48:51]\n\t"
      "ds_read_b128 a[244:247], v34 offset:18432\n\t"
      "v_mfma_f32_16x16x32_bf16 v[52:55], a[192:195], a[248:251], v[52:55]\n\t"
      "v_mfma_f32_16x16x32_bf16 v[56:59], a[200:203], a[248:251], v[56:59]\n\t"
      "ds_read2_b32 v[2:3], v4 offset0:4 offset1:6\n\t"
      "buffer_load_dwordx4 a[12:15], v12, s[52:55], s69 offen offset:1024 sc1\n\t"
      "v_mfma_f32_16x16x32_bf16 v[60:63], a[208:211], a[248:251], v[60:63]\n\t"
      "s_waitcnt vmcnt(11) lgkmcnt(5)\n\t"
      "ds_read_b128 a[248:251], v35 offset:18432\n\t"
      "v_mfma_f32_16x16x32_bf16 v[108:111], a[196:199], a[252:255], v[108:111]\n\t"
      "v_mfma_f32_16x16x32_bf16 v[144:147], a[204:207], a[252:255], v[144:147]\n\t"
      "v_mfma_f32_16x16x32_bf16 v[184:187], a[212:215], a[252:255], v[184:187]\n\t"
      "ds_read_b128 a[252:255], v33 offset:19456\n\t"
      "buffer_load_dwordx4 a[20:23], v12, s[52:55], s69 offen offset:2048 sc1\n\t"
      "v_mfma_f32_16x16x32_bf16 v[40:43], a[196:199], a[88:91], v[40:43]\n\t"
      "s_add_u32 s69, s69, 0xc00\n\t"
      "v_mfma_f32_16x16x32_bf16 v[44:47], a[204:207], a[88:91], v[44:47]\n\t"
      "s_nop 0\n\t"
      "v_cvt_pk_bf16_f32 v108, v108, v109\n\t"
      "v_cvt_pk_bf16_f32 v109, v110, v111\n\t"
      "v_mfma_f32_16x16x32_bf16 v[48:51], a[212:215], a[88:91], v[48:51]\n\t"
      "ds_read_b128 a[88:91], v34 offset:19456\n\t"
      "v_cvt_pk_bf16_f32 v144, v144, v145\n\t"
      "v_mfma_f32_16x16x32_bf16 v[52:55], a[196:199], a[92:95], v[52:55]\n\t"
      "v_cvt_pk_bf16_f32 v184, v184, v185\n\t"
      "v_cvt_pk_bf16_f32 v40, v40, v41\n\t"
      "v_mfma_f32_16x16x32_bf16 v[56:59], a[204:207], a[92:95], v[56:59]\n\t"
      "buffer_load_dwordx4 a[24:27], v12, s[52:55], s69 offen sc1\n\t"
      "v_cvt_pk_bf16_f32 v44, v44, v45\n\t"
      "v_mfma_f32_16x16x32_bf16 v[60:63], a[212:215], a[92:95], v[60:63]\n\t"
      "ds_read_b128 a[92:95], v35 offset:19456\n\t"
      "v_cvt_pk_bf16_f32 v48, v48, v49\n\t"
      "v_smfmac_f32_16x16x64_bf16 v[188:191], v[8:11], a[192:199], v236\n\t"
      "v_cvt_pk_bf16_f32 v52, v52, v53\n\t"
      "v_cvt_pk_bf16_f32 v145, v146, v147\n\t"
      "v_smfmac_f32_16x16x64_bf16 v[192:195], v[8:11], a[200:207], v236\n\t"
      "v_cvt_pk_bf16_f32 v56, v56, v57\n\t"
      "buffer_load_dwordx4 a[32:35], v12, s[52:55], s69 offen offset:1024 sc1\n\t"
      "v_smfmac_f32_16x16x64_bf16 v[196:199], v[8:11], a[208:215], v236\n\t"
      "ds_read2_b32 v[8:9], v4 offset0:32 offset1:34\n\t"
      "v_cvt_pk_bf16_f32 v60, v60, v61\n\t"
      "v_mfma_f32_16x16x32_bf16 v[200:203], a[154:157], a[72:75], 0\n\t"
      "ds_read2_b32 v[10:11], v4 offset0:36 offset1:38\n\t"
      "v_cvt_pk_bf16_f32 v185, v186, v187\n\t"
      "v_mfma_f32_16x16x32_bf16 v[204:207], a[170:173], a[72:75], 0\n\t"
      "v_cvt_pk_bf16_f32 v41, v42, v43\n\t"
      "v_cvt_pk_bf16_f32 v45, v46, v47\n\t"
      "v_mfma_f32_16x16x32_bf16 v[208:211], a[186:189], a[72:75], 0\n\t"
      "s_waitcnt vmcnt(11) lgkmcnt(4)\n\t"
      "buffer_load_dwordx4 a[40:43], v12, s[52:55], s69 offen offset:2048 sc1\n\t"
      "v_mfma_f32_16x16x32_bf16 v[148:151], a[216:219], a[240:243], v[148:151]\n\t"
      "s_add_u32 s69, s69, 0xc00\n\t"
      "v_cvt_pk_bf16_f32 v49, v50, v51\n\t"
      "v_cvt_pk_bf16_f32 v53, v54, v55\n\t"
      "v_mfma_f32_16x16x32_bf16 v[152:155], a[224:227], a[240:243], v[152:155]\n\t"
      "v_cvt_pk_bf16_f32 v57, v58, v59\n\t"
      "v_cvt_pk_bf16_f32 v61, v62, v63\n\t"
      "v_mfma_f32_16x16x32_bf16 v[156:159], a[232:235], a[240:243], v[156:159]\n\t"
      "ds_read_b128 a[240:243], v33 offset:20480\n\t"
      "v_cvt_pk_bf16_f32 v200, v200, v201\n\t"
      "v_mfma_f32_16x16x32_bf16 v[160:163], a[216:219], a[244:247], v[160:163]\n\t"
      "ds_write_b64 v239, v[108:109]\n\t"
      "v_mfma_f32_16x16x32_bf16 v[164:167], a[224:227], a[244:247], v[164:167]\n\t"
      "buffer_load_dwordx4 a[28:31], v12, s[52:55], s69 offen sc1\n\t"
      "v_cvt_pk_bf16_f32 v204, v204, v205\n\t"
      "v_mfma_f32_16x16x32_bf16 v[168:171], a[232:235], a[244:247], v[168:171]\n\t"
      "ds_read_b128 a[244:247], v34 offset:20480\n\t"
      "v_cvt_pk_bf16_f32 v208, v208, v209\n\t"
      "v_mfma_f32_16x16x32_bf16 v[172:175], a[216:219], a[248:251], v[172:175]\n\t"
      "ds_write_b64 v239, v[144:145] offset:8\n\t"
      "v_mfma_f32_16x16x32_bf16 v[176:179], a[224:227], a[248:251], v[176:179]\n\t"
      "buffer_load_dwordx4 a[36:39], v12, s[52:55], s69 offen offset:1024 sc1\n\t"
      "v_cvt_pk_bf16_f32 v201, v202, v203\n\t"
      "v_mfma_f32_16x16x32_bf16 v[180:183], a[232:235], a[248:251], v[180:183]\n\t"
      "s_waitcnt vmcnt(11)\n\t"
      "ds_read_b128 a[248:251], v35 offset:20480\n\t"
      "v_mfma_f32_16x16x32_bf16 v[148:151], a[220:223], a[252:255], v[148:151]\n\t"
      "ds_write_b64 v243, v[184:185]\n\t"
      "v_mfma_f32_16x16x32_bf16 v[152:155], a[228:231], a[252:255], v[152:155]\n\t"
      "v_cvt_pk_bf16_f32 v205, v206, v207\n\t"
      "v_cvt_pk_bf16_f32 v209, v210, v211\n\t"
      "v_mfma_f32_16x16x32_bf16 v[156:159], a[236:239], a[252:255], v[156:159]\n\t"
      "s_waitcnt lgkmcnt(6)\n\t"
      "buffer_load_dwordx4 a[44:47], v12, s[52:55], s69 offen offset:2048 sc1\n\t"
      "v_mfma_f32_16x16x32_bf16 v[160:163], a[220:223], a[88:91], v[160:163]\n\t"
      "s_add_u32 s69, s69, 0xc00\n\t"
      "ds_read_b128 a[252:255], v33 offset:21504\n\t"
      "v_mfma_f32_16x16x32_bf16 v[164:167], a[228:231], a[88:91], v[164:167]\n\t"
      "ds_write_b64 v240, v[40:41]\n\t"
      "v_mfma_f32_16x16x32_bf16 v[168:171], a[236:239], a[88:91], v[168:171]\n\t"
      "v_mfma_f32_16x16x32_bf16 v[172:175], a[220:223], a[92:95], v[172:175]\n\t"
      "ds_read_b128 a[88:91], v34 offset:21504\n\t"
      "v_mfma_f32_16x16x32_bf16 v[176:179], a[228:231], a[92:95], v[176:179]\n\t"
      "buffer_load_dwordx4 a[48:51], v12, s[52:55], s69 offen sc1\n\t"
      "v_mfma_f32_16x16x32_bf16 v[180:183], a[236:239], a[92:95], v[180:183]\n\t"
      "ds_read_b128 a[92:95], v35 offset:21504\n\t"
      "v_smfmac_f32_16x16x64_bf16 v[188:191], v[0:3], a[216:223], v236\n\t"
      "ds_write_b64 v240, v[44:45] offset:8\n\t"
      "v_smfmac_f32_16x16x64_bf16 v[192:195], v[0:3], a[224:231], v236\n\t"
      "buffer_load_dwordx4 a[56:59], v12, s[52:55], s69 offen offset:1024 sc1\n\t"
      "v_smfmac_f32_16x16x64_bf16 v[196:199], v[0:3], a[232:239], v236\n\t"
      "s_waitcnt vmcnt(11) lgkmcnt(5)\n\t"
      "ds_read2_b32 v[0:1], v4 offset0:64 offset1:66\n\t"
      "v_mfma_f32_16x16x32_bf16 v[148:151], a[0:3], a[240:243], v[148:151]\n\t"
      "ds_read2_b32 v[2:3], v4 offset0:68 offset1:70\n\t"
      "v_bfe_u32 v4, v237, 28, 2\n\t"
      "v_mfma_f32_16x16x32_bf16 v[152:155], a[8:11], a[240:243], v[152:155]\n\t"
      "ds_write_b64 v244, v[48:49]\n\t"
      "v_mfma_f32_16x16x32_bf16 v[156:159], a[16:19], a[240:243], v[156:159]\n\t"
      "buffer_load_dwordx4 a[64:67], v12, s[52:55], s69 offen offset:2048 sc1\n\t"
      "s_add_u32 s69, s69, 0xc00\n\t"
      "v_mfma_f32_16x16x32_bf16 v[160:163], a[0:3], a[244:247], v[160:163]\n\t"
      "ds_read_b128 a[240:243], v33 offset:22528\n\t"
      "v_mad_u32_u24 v4, v4, s76, v238\n\t"
      "v_mfma_f32_16x16x32_bf16 v[164:167], a[8:11], a[244:247], v[164:167]\n\t"
      "ds_write_b64 v241, v[52:53]\n\t"
      "v_mfma_f32_16x16x32_bf16 v[168:171], a[16:19], a[244:247], v[168:171]\n\t"
      "v_add_u32_e32 v4, 0x900, v4\n\t"
      "v_mfma_f32_16x16x32_bf16 v[172:175], a[0:3], a[248:251], v[172:175]\n\t"
      "buffer_load_dwordx4 a[52:55], v12, s[52:55], s69 offen sc1\n\t"
      "v_mfma_f32_16x16x32_bf16 v[176:179], a[8:11], a[248:251], v[176:179]\n\t"
      "ds_read_b128 a[244:247], v34 offset:22528\n\t"
      "v_mfma_f32_16x16x32_bf16 v[180:183], a[16:19], a[248:251], v[180:183]\n\t"
      "ds_write_b64 v241, v[56:57] offset:8\n\t"
      "v_mfma_f32_16x16x32_bf16 v[212:215], a[156:159], a[72:75], 0\n\t"
      "v_mfma_f32_16x16x32_bf16 v[216:219], a[172:175], a[72:75], 0\n\t"
      "buffer_load_dwordx4 a[60:63], v12, s[52:55], s69 offen offset:1024 sc1\n\t"
      "v_mfma_f32_16x16x32_bf16 v[220:223], a[188:191], a[72:75], 0\n\t"
      "s_waitcnt vmcnt(11) lgkmcnt(4)\n\t"
      "ds_read_b128 a[248:251], v35 offset:22528\n\t"
      "v_mfma_f32_16x16x32_bf16 v[148:151], a[4:7], a[252:255], v[148:151]\n\t"
      "ds_write_b64 v245, v[60:61]\n\t"
      "v_mfma_f32_16x16x32_bf16 v[152:155], a[12:15], a[252:255], v[152:155]\n\t"
      "v_cvt_pk_bf16_f32 v212, v212, v213\n\t"
      "buffer_load_dwordx4 a[68:71], v12, s[52:55], s69 offen offset:2048 sc1\n\t"
      "v_mfma_f32_16x16x32_bf16 v[156:159], a[20:23], a[252:255], v[156:159]\n\t"
      "s_add_u32 s69, s69, 0xc00\n\t"
      "ds_read_b128 a[252:255], v33 offset:23552\n\t"
      "v_mfma_f32_16x16x32_bf16 v[160:163], a[4:7], a[88:91], v[160:163]\n\t"
      "ds_write_b64 v18, v[200:201] offset:36864\n\t"
      "v_mfma_f32_16x16x32_bf16 v[164:167], a[12:15], a[88:91], v[164:167]\n\t"
      "v_cvt_pk_bf16_f32 v216, v216, v217\n\t"
      "v_cvt_pk_bf16_f32 v220, v220, v221\n\t"
      "v_mfma_f32_16x16x32_bf16 v[168:171], a[20:23], a[88:91], v[168:171]\n\t"
      "ds_read_b128 a[88:91], v34 offset:23552\n\t"
      "buffer_load_dwordx4 a[192:195], v12, s[52:55], s69 offen sc1\n\t"
      "v_mfma_f32_16x16x32_bf16 v[172:175], a[4:7], a[92:95], v[172:175]\n\t"
      "ds_write_b64 v18, v[204:205] offset:36872\n\t"
      "v_mfma_f32_16x16x32_bf16 v[176:179], a[12:15], a[92:95], v[176:179]\n\t"
      "v_cvt_pk_bf16_f32 v213, v214, v215\n\t"
      "v_cvt_pk_bf16_f32 v217, v218, v219\n\t"
      "v_mfma_f32_16x16x32_bf16 v[180:183], a[20:23], a[92:95], v[180:183]\n\t"
      "ds_read_b128 a[92:95], v35 offset:23552\n\t"
      "v_cvt_pk_bf16_f32 v221, v222, v223\n\t"
      "v_smfmac_f32_16x16x64_bf16 v[188:191], v[8:11], a[0:7], v236\n\t"
      "buffer_load_dwordx4 a[200:203], v12, s[52:55], s69 offen offset:1024 sc1\n\t"
      "v_smfmac_f32_16x16x64_bf16 v[192:195], v[8:11], a[8:15], v236\n\t"
      "ds_write_b64 v25, v[208:209] offset:36864\n\t"
      "v_smfmac_f32_16x16x64_bf16 v[196:199], v[8:11], a[16:23], v236\n\t"
      "s_waitcnt vmcnt(11) lgkmcnt(6)\n\t"
      "v_mfma_f32_16x16x32_bf16 v[148:151], a[24:27], a[240:243], v[148:151]\n\t"
      "ds_read2_b32 v[8:9], v5 offset0:0 offset1:2\n\t"
      "buffer_load_dwordx4 a[208:211], v12, s[52:55], s69 offen offset:2048 sc1\n\t"
      "v_mfma_f32_16x16x32_bf16 v[152:155], a[32:35], a[240:243], v[152:155]\n\t"
      "s_add_u32 s69, s69, 0xc00\n\t"
      "ds_read2_b32 v[10:11], v5 offset0:4 offset1:6\n\t"
      "v_mfma_f32_16x16x32_bf16 v[156:159], a[40:43], a[240:243], v[156:159]\n\t"
      "ds_read_b128 a[240:243], v33 offset:30720\n\t"
      "v_mfma_f32_16x16x32_bf16 v[160:163], a[24:27], a[244:247], v[160:163]\n\t"
      "ds_write_b64 v29, v[212:213]\n\t"
      "v_mfma_f32_16x16x32_bf16 v[164:167], a[32:35], a[244:247], v[164:167]\n\t"
      "v_mfma_f32_16x16x32_bf16 v[168:171], a[40:43], a[244:247], v[168:171]\n\t"
      "buffer_load_dwordx4 a[196:199], v12, s[52:55], s69 offen sc1\n\t"
      "v_mfma_f32_16x16x32_bf16 v[172:175], a[24:27], a[248:251], v[172:175]\n\t"
      "ds_read_b128 a[244:247], v34 offset:30720\n\t"
      "v_mfma_f32_16x16x32_bf16 v[176:179], a[32:35], a[248:251], v[176:179]\n\t"
      "ds_write_b64 v29, v[216:217] offset:8\n\t"
      "v_mfma_f32_16x16x32_bf16 v[180:183], a[40:43], a[248:251], v[180:183]\n\t"
      "s_barrier\n\t"
      "s_waitcnt vmcnt(10) lgkmcnt(6)\n\t"
      "buffer_load_dwordx4 a[204:207], v12, s[52:55], s69 offen offset:1024 sc1\n\t"
      "v_mfma_f32_16x16x32_bf16 v[148:151], a[28:31], a[252:255], v[148:151]\n\t"
      "ds_read_b128 a[248:251], v35 offset:30720\n\t"
      "v_mfma_f32_16x16x32_bf16 v[152:155], a[36:39], a[252:255], v[152:155]\n\t"
      "ds_write_b64 v32, v[220:221]\n\t"
      "v_mfma_f32_16x16x32_bf16 v[156:159], a[44:47], a[252:255], v[156:159]\n\t"
      "v_mfma_f32_16x16x32_bf16 v[160:163], a[28:31], a[88:91], v[160:163]\n\t"
      "buffer_load_dwordx4 a[212:215], v12, s[52:55], s69 offen offset:2048 sc1\n\t"
      "s_add_u32 s69, s69, 0xc00\n\t"
      "v_mfma_f32_16x16x32_bf16 v[164:167], a[36:39], a[88:91], v[164:167]\n\t"
      "ds_read_b128 a[252:255], v33 offset:31744\n\t"
      "v_add_f32_e32 v64, v72, v148\n\t"
      "v_mfma_f32_16x16x32_bf16 v[168:171], a[44:47], a[88:91], v[168:171]\n\t"
      "v_add_f32_e32 v65, v73, v149\n\t"
      "v_add_f32_e32 v66, v74, v150\n\t"
      "v_mfma_f32_16x16x32_bf16 v[172:175], a[28:31], a[92:95], v[172:175]\n\t"
      "ds_read_b128 a[88:91], v34 offset:31744\n\t"
      "v_add_f32_e32 v67, v75, v151\n\t"
      "v_mfma_f32_16x16x32_bf16 v[176:179], a[36:39], a[92:95], v[176:179]\n\t"
      "buffer_load_dwordx4 a[216:219], v12, s[52:55], s69 offen sc1\n\t"
      "v_sub_f32_e32 v148, v72, v148\n\t"
      "v_mfma_f32_16x16x32_bf16 v[180:183], a[44:47], a[92:95], v[180:183]\n\t"
      "ds_read_b128 a[92:95], v35 offset:31744\n\t"
      "v_sub_f32_e32 v149, v73, v149\n\t"
      "v_smfmac_f32_16x16x64_bf16 v[188:191], v[0:3], a[24:31], v236\n\t"
      "v_sub_f32_e32 v150, v74, v150\n\t"
      "v_sub_f32_e32 v151, v75, v151\n\t"
      "v_smfmac_f32_16x16x64_bf16 v[192:195], v[0:3], a[32:39], v236\n\t"
      "ds_read_b128 v[108:111], v247\n\t"
      "v_add_f32_e32 v72, v112, v152\n\t"
      "v_smfmac_f32_16x16x64_bf16 v[196:199], v[0:3], a[40:47], v236\n\t"
      "s_waitcnt vmcnt(10) lgkmcnt(5)\n\t"
      "buffer_load_dwordx4 a[224:227], v12, s[52:55], s69 offen offset:1024 sc1\n\t"
      "v_mfma_f32_16x16x32_bf16 v[68:71], a[48:51], a[240:243], v[68:71]\n\t"
      "ds_read2_b32 v[0:1], v5 offset0:32 offset1:34\n\t"
      "v_add_f32_e32 v73, v113, v153\n\t"
      "v_mfma_f32_16x16x32_bf16 v[76:79], a[56:59], a[240:243], v[76:79]\n\t"
      "ds_read2_b32 v[2:3], v5 offset0:36 offset1:38\n\t"
      "v_add_f32_e32 v74, v114, v154\n\t"
      "v_mfma_f32_16x16x32_bf16 v[80:83], a[64:67], a[240:243], v[80:83]\n\t"
      "ds_read_b128 a[240:243], v33 offset:32768\n\t"
      "v_add_f32_e32 v75, v115, v155\n\t"
      "v_mfma_f32_16x16x32_bf16 v[84:87], a[48:51], a[244:247], v[84:87]\n\t"
      "buffer_load_dwordx4 a[232:235], v12, s[52:55], s69 offen offset:2048 sc1\n\t"
      "s_add_u32 s69, s69, 0xc00\n\t"
      "v_mfma_f32_16x16x32_bf16 v[88:91], a[56:59], a[244:247], v[88:91]\n\t"
      "ds_read_b128 v[40:43], v248\n\t"
      "v_sub_f32_e32 v152, v112, v152\n\t"
      "v_mfma_f32_16x16x32_bf16 v[92:95], a[64:67], a[244:247], v[92:95]\n\t"
      "v_sub_f32_e32 v153, v113, v153\n\t"
      "v_sub_f32_e32 v154, v114, v154\n\t"
      "v_mfma_f32_16x16x32_bf16 v[96:99], a[48:51], a[248:251], v[96:99]\n\t"
      "ds_read_b128 a[244:247], v34 offset:32768\n\t"
      "v_sub_f32_e32 v155, v115, v155\n\t"
      "v_mfma_f32_16x16x32_bf16 v[100:103], a[56:59], a[248:251], v[100:103]\n\t"
      "s_waitcnt lgkmcnt(5)\n\t"
      "buffer_store_dwordx4 v[108:111], v252, s[48:51], s71 offen nt\n\t"
      "v_mfma_f32_16x16x32_bf16 v[104:107], a[64:67], a[248:251], v[104:107]\n\t"
      "s_waitcnt vmcnt(10)\n\t"
      "ds_read_b128 a[248:251], v35 offset:32768\n\t"
      "v_mfma_f32_16x16x32_bf16 v[68:71], a[52:55], a[252:255], v[68:71]\n\t"
      "v_add_f32_e32 v112, v116, v156\n\t"
      "v_add_f32_e32 v113, v117, v157\n\t"
      "v_mfma_f32_16x16x32_bf16 v[76:79], a[60:63], a[252:255], v[76:79]\n\t"
      "ds_read_b128 v[52:55], v249\n\t"
      "buffer_load_dwordx4 a[220:223], v12, s[52:55], s69 offen sc1\n\t"
      "v_mfma_f32_16x16x32_bf16 v[80:83], a[68:71], a[252:255], v[80:83]\n\t"
      "v_add_f32_e32 v114, v118, v158\n\t"
      "v_add_f32_e32 v115, v119, v159\n\t"
      "v_mfma_f32_16x16x32_bf16 v[84:87], a[52:55], a[88:91], v[84:87]\n\t"
      "ds_read_b128 a[252:255], v33 offset:33792\n\t"
      "v_sub_f32_e32 v156, v116, v156\n\t"
      "v_mfma_f32_16x16x32_bf16 v[88:91], a[60:63], a[88:91], v[88:91]\n\t"
      "v_sub_f32_e32 v157, v117, v157\n\t"
      "v_sub_f32_e32 v158, v118, v158\n\t"
      "v_mfma_f32_16x16x32_bf16 v[92:95], a[68:71], a[88:91], v[92:95]\n\t"
      "buffer_load_dwordx4 a[228:231], v12, s[52:55], s69 offen offset:1024 sc1\n\t"
      "v_sub_f32_e32 v159, v119, v159\n\t"
      "v_mfma_f32_16x16x32_bf16 v[96:99], a[52:55], a[92:95], v[96:99]\n\t"
      "ds_read_b128 a[88:91], v34 offset:33792\n\t"
      "v_add_f32_e32 v116, v120, v160\n\t"
      "v_mfma_f32_16x16x32_bf16 v[100:103], a[60:63], a[92:95], v[100:103]\n\t"
      "v_add_f32_e32 v117, v121, v161\n\t"
      "v_add_f32_e32 v118, v122, v162\n\t"
      "v_mfma_f32_16x16x32_bf16 v[104:107], a[68:71], a[92:95], v[104:107]\n\t"
      "ds_read_b128 a[92:95], v35 offset:33792\n\t"
      "v_add_f32_e32 v119, v123, v163\n\t"
      "v_smfmac_f32_16x16x64_bf16 v[188:191], v[8:11], a[48:55], v236\n\t"
      "buffer_load_dwordx4 a[236:239], v12, s[52:55], s69 offen offset:2048 sc1\n\t"
      "s_add_u32 s69, s69, 0xc00\n\t"
      "v_smfmac_f32_16x16x64_bf16 v[192:195], v[8:11], a[56:63], v236\n\t"
      "ds_read_b128 v[144:147], v250\n\t"
      "v_sub_f32_e32 v160, v120, v160\n\t"
      "v_smfmac_f32_16x16x64_bf16 v[196:199], v[8:11], a[64:71], v236\n\t"
      "s_waitcnt vmcnt(10) lgkmcnt(5)\n\t"
      "v_sub_f32_e32 v161, v121, v161\n\t"
      "v_sub_f32_e32 v162, v122, v162\n\t"
      "v_mfma_f32_16x16x32_bf16 v[68:71], a[192:195], a[240:243], v[68:71]\n\t"
      "ds_read2_b32 v[8:9], v5 offset0:64 offset1:66\n\t"
      "buffer_load_dwordx4 a[0:3], v12, s[52:55], s69 offen sc1\n\t"
      "v_mfma_f32_16x16x32_bf16 v[76:79], a[200:203], a[240:243], v[76:79]\n\t"
      "ds_read2_b32 v[10:11], v5 offset0:68 offset1:70\n\t"
      "v_sub_f32_e32 v163, v123, v163\n\t"
      "v_mfma_f32_16x16x32_bf16 v[80:83], a[208:211], a[240:243], v[80:83]\n\t"
      "ds_read_b128 a[240:243], v33 offset:34816\n\t"
      "v_add_f32_e32 v120, v124, v164\n\t"
      "v_mfma_f32_16x16x32_bf16 v[84:87], a[192:195], a[244:247], v[84:87]\n\t"
      "v_add_f32_e32 v121, v125, v165\n\t"
      "v_add_f32_e32 v122, v126, v166\n\t"
      "v_mfma_f32_16x16x32_bf16 v[88:91], a[200:203], a[244:247], v[88:91]\n\t"
      "buffer_load_dwordx4 a[8:11], v12, s[52:55], s69 offen offset:1024 sc1\n\t"
      "v_add_f32_e32 v123, v127, v167\n\t"
      "v_mfma_f32_16x16x32_bf16 v[92:95], a[208:211], a[244:247], v[92:95]\n\t"
      "ds_read_b128 a[244:247], v34 offset:34816\n\t"
      "v_sub_f32_e32 v164, v124, v164\n\t"
      "v_mfma_f32_16x16x32_bf16 v[96:99], a[192:195], a[248:251], v[96:99]\n\t"
      "v_sub_f32_e32 v165, v125, v165\n\t"
      "v_sub_f32_e32 v166, v126, v166\n\t"
      "v_mfma_f32_16x16x32_bf16 v[100:103], a[200:203], a[248:251], v[100:103]\n\t"
      "ds_read_b128 v[44:47], v251\n\t"
      "v_sub_f32_e32 v167, v127, v167\n\t"
      "v_mfma_f32_16x16x32_bf16 v[104:107], a[208:211], a[248:251], v[104:107]\n\t"
      "s_waitcnt vmcnt(9) lgkmcnt(6)\n\t"
      "buffer_load_dwordx4 a[16:19], v12, s[52:55], s69 offen offset:2048 sc1\n\t"
      "v_mfma_f32_16x16x32_bf16 v[68:71], a[196:199], a[252:255], v[68:71]\n\t"
      "s_add_u32 s69, s69, 0xc00\n\t"
      "ds_read_b128 a[248:251], v35 offset:34816\n\t"
      "v_mfma_f32_16x16x32_bf16 v[76:79], a[204:207], a[252:255], v[76:79]\n\t"
      "v_add_f32_e32 v124, v128, v168\n\t"
      "v_add_f32_e32 v125, v129, v169\n\t"
      "v_mfma_f32_16x16x32_bf16 v[80:83], a[212:215], a[252:255], v[80:83]\n\t"
      "ds_read_b128 a[252:255], v33 offset:35840\n\t"
      "buffer_load_dwordx4 a[4:7], v12, s[52:55], s69 offen sc1\n\t"
      "v_mfma_f32_16x16x32_bf16 v[84:87], a[196:199], a[88:91], v[84:87]\n\t"
      "v_add_f32_e32 v126, v130, v170\n\t"
      "v_add_f32_e32 v127, v131, v171\n\t"
      "v_mfma_f32_16x16x32_bf16 v[88:91], a[204:207], a[88:91], v[88:91]\n\t"
      "v_sub_f32_e32 v168, v128, v168\n\t"
      "v_sub_f32_e32 v169, v129, v169\n\t"
      "v_mfma_f32_16x16x32_bf16 v[92:95], a[212:215], a[88:91], v[92:95]\n\t"
      "ds_read_b128 a[88:91], v34 offset:35840\n\t"
      "v_sub_f32_e32 v170, v130, v170\n\t"
      "v_mfma_f32_16x16x32_bf16 v[96:99], a[196:199], a[92:95], v[96:99]\n\t"
      "buffer_load_dwordx4 a[12:15], v12, s[52:55], s69 offen offset:1024 sc1\n\t"
      "v_sub_f32_e32 v171, v131, v171\n\t"
      "v_mfma_f32_16x16x32_bf16 v[100:103], a[204:207], a[92:95], v[100:103]\n\t"
      "v_add_f32_e32 v128, v132, v172\n\t"
      "v_add_f32_e32 v129, v133, v173\n\t"
      "v_mfma_f32_16x16x32_bf16 v[104:107], a[212:215], a[92:95], v[104:107]\n\t"
      "ds_read_b128 a[92:95], v35 offset:35840\n\t"
      "v_add_f32_e32 v130, v134, v174\n\t"
      "v_smfmac_f32_16x16x64_bf16 v[188:191], v[0:3], a[192:199], v236\n\t"
      "v_add_f32_e32 v131, v135, v175\n\t"
      "v_sub_f32_e32 v172, v132, v172\n\t"
      "v_smfmac_f32_16x16x64_bf16 v[192:195], v[0:3], a[200:207], v236\n\t"
      "buffer_load_dwordx4 a[20:23], v12, s[52:55], s69 offen offset:2048 sc1\n\t"
      "s_add_u32 s69, s69, 0xc00\n\t"
      "v_smfmac_f32_16x16x64_bf16 v[196:199], v[0:3], a[208:215], v236\n\t"
      "s_barrier\n\t"
      "s_waitcnt vmcnt(10) lgkmcnt(3)\n\t"
      "v_sub_f32_e32 v173, v133, v173\n\t"
      "v_mfma_f32_16x16x32_bf16 v[68:71], a[216:219], a[240:243], v[68:71]\n\t"
      "ds_read2_b32 v[0:1], v4 offset0:0 offset1:2\n\t"
      "v_sub_f32_e32 v174, v134, v174\n\t"
      "v_mfma_f32_16x16x32_bf16 v[76:79], a[224:227], a[240:243], v[76:79]\n\t"
      "ds_read2_b32 v[2:3], v4 offset0:4 offset1:6\n\t"
      "v_sub_f32_e32 v175, v135, v175\n\t"
      "v_mfma_f32_16x16x32_bf16 v[80:83], a[232:235], a[240:243], v[80:83]\n\t"
      "ds_read_b128 a[240:243], v33 offset:36864\n\t"
      "v_add_f32_e32 v132, v136, v176\n\t"
      "v_mfma_f32_16x16x32_bf16 v[84:87], a[216:219], a[244:247], v[84:87]\n\t"
      "buffer_store_dwordx4 v[40:43], v253, s[48:51], s71 offen nt\n\t"
      "v_add_f32_e32 v133, v137, v177\n\t"
      "v_mfma_f32_16x16x32_bf16 v[88:91], a[224:227], a[244:247], v[88:91]\n\t"
      "buffer_store_dwordx4 v[52:55], v254, s[48:51], s71 offen nt\n\t"
      "buffer_store_dwordx4 v[144:147], v36, s[48:51], s71 offen nt\n\t"
      "v_mfma_f32_16x16x32_bf16 v[92:95], a[232:235], a[244:247], v[92:95]\n\t"
      "buffer_store_dwordx4 v[44:47], v27, s[48:51], s71 offen nt\n\t"
      "v_add_f32_e32 v134, v138, v178\n\t"
      "v_mfma_f32_16x16x32_bf16 v[96:99], a[216:219], a[248:251], v[96:99]\n\t"
      "ds_read_b128 a[244:247], v34 offset:36864\n\t"
      "v_add_f32_e32 v135, v139, v179\n\t"
      "v_mfma_f32_16x16x32_bf16 v[100:103], a[224:227], a[248:251], v[100:103]\n\t"
      "v_sub_f32_e32 v176, v136, v176\n\t"
      "v_sub_f32_e32 v177, v137, v177\n\t"
      "v_mfma_f32_16x16x32_bf16 v[104:107], a[232:235], a[248:251], v[104:107]\n\t"
      "s_waitcnt vmcnt(10) lgkmcnt(4)\n\t"
      "ds_read_b128 a[248:251], v35 offset:36864\n\t"
      "v_mfma_f32_16x16x32_bf16 v[68:71], a[220:223], a[252:255], v[68:71]\n\t"
      "v_sub_f32_e32 v178, v138, v178\n\t"
      "v_sub_f32_e32 v179, v139, v179\n\t"
      "v_mfma_f32_16x16x32_bf16 v[76:79], a[228:231], a[252:255], v[76:79]\n\t"
      "v_add_f32_e32 v136, v140, v180\n\t"
      "v_add_f32_e32 v137, v141, v181\n\t"
      "v_add_f32_e32 v138, v142, v182\n\t"
      "v_add_f32_e32 v139, v143, v183\n\t"
      "v_sub_f32_e32 v180, v140, v180\n\t"
      "v_sub_f32_e32 v181, v141, v181\n\t"
      "v_sub_f32_e32 v182, v142, v182\n\t"
      "v_sub_f32_e32 v183, v143, v183\n\t"
      "v_cvt_pk_bf16_f32 v68, v68, v69\n\t"
      "v_cvt_pk_bf16_f32 v76, v76, v77\n\t"
      "v_cvt_pk_bf16_f32 v69, v70, v71\n\t"
      "v_mfma_f32_16x16x32_bf16 v[80:83], a[236:239], a[252:255], v[80:83]\n\t"
      "ds_read_b128 a[252:255], v33 offset:37888\n\t"
      "v_cvt_pk_bf16_f32 v77, v78, v79\n\t"
      "v_mfma_f32_16x16x32_bf16 v[84:87], a[220:223], a[88:91], v[84:87]\n\t"
      "ds_write_b64 v239, v[68:69] offset:6144\n\t"
      "v_mfma_f32_16x16x32_bf16 v[88:91], a[228:231], a[88:91], v[88:91]\n\t"
      "v_bfe_u32 v5, v237, 30, 2\n\t"
      "v_mfma_f32_16x16x32_bf16 v[92:95], a[236:239], a[88:91], v[92:95]\n\t"
      "ds_read_b128 a[88:91], v34 offset:37888\n\t"
      "v_cvt_pk_bf16_f32 v80, v80, v81\n\t"
      "v_mfma_f32_16x16x32_bf16 v[96:99], a[220:223], a[92:95], v[96:99]\n\t"
      "buffer_load_dwordx4 a[24:27], v12, s[52:55], s69 offen sc1\n\t"
      "v_cvt_pk_bf16_f32 v84, v84, v85\n\t"
      "v_mfma_f32_16x16x32_bf16 v[100:103], a[228:231], a[92:95], v[100:103]\n\t"
      "v_cvt_pk_bf16_f32 v88, v88, v89\n\t"
      "v_cvt_pk_bf16_f32 v81, v82, v83\n\t"
      "v_mfma_f32_16x16x32_bf16 v[104:107], a[236:239], a[92:95], v[104:107]\n\t"
      "ds_read_b128 a[92:95], v35 offset:37888\n\t"
      "v_cvt_pk_bf16_f32 v92, v92, v93\n\t"
      "v_smfmac_f32_16x16x64_bf16 v[188:191], v[8:11], a[216:223], v236\n\t"
      "v_cvt_pk_bf16_f32 v96, v96, v97\n\t"
      "buffer_load_dwordx4 a[32:35], v12, s[52:55], s69 offen offset:1024 sc1\n\t"
      "v_smfmac_f32_16x16x64_bf16 v[192:195], v[8:11], a[224:231], v236\n\t"
      "v_cvt_pk_bf16_f32 v100, v100, v101\n\t"
      "v_cvt_pk_bf16_f32 v85, v86, v87\n\t"
      "v_smfmac_f32_16x16x64_bf16 v[196:199], v[8:11], a[232:239], v236\n\t"
      "s_waitcnt vmcnt(9) lgkmcnt(4)\n\t"
      "ds_read2_b32 v[8:9], v4 offset0:32 offset1:34\n\t"
      "v_mfma_f32_16x16x32_bf16 v[64:67], a[0:3], a[240:243], v[64:67]\n\t"
      "ds_read2_b32 v[10:11], v4 offset0:36 offset1:38\n\t"
      "v_cvt_pk_bf16_f32 v104, v104, v105\n\t"
      "v_mfma_f32_16x16x32_bf16 v[72:75], a[8:11], a[240:243], v[72:75]\n\t"
      "buffer_load_dwordx4 a[40:43], v12, s[52:55], s69 offen offset:2048 sc1\n\t"
      "s_add_u32 s69, s69, 0xc00\n\t"
      "v_mfma_f32_16x16x32_bf16 v[112:115], a[16:19], a[240:243], v[112:115]\n\t"
      "ds_read_b128 a[240:243], v33 offset:38912\n\t"
      "v_cvt_pk_bf16_f32 v89, v90, v91\n\t"
      "v_mfma_f32_16x16x32_bf16 v[116:119], a[0:3], a[244:247], v[116:119]\n\t"
      "v_cvt_pk_bf16_f32 v93, v94, v95\n\t"
      "v_cvt_pk_bf16_f32 v97, v98, v99\n\t"
      "v_mfma_f32_16x16x32_bf16 v[120:123], a[8:11], a[244:247], v[120:123]\n\t"
      "v_cvt_pk_bf16_f32 v101, v102, v103\n\t"
      "buffer_load_dwordx4 a[28:31], v12, s[52:55], s69 offen sc1\n\t"
      "v_mfma_f32_16x16x32_bf16 v[124:127], a[16:19], a[244:247], v[124:127]\n\t"
      "ds_read_b128 a[244:247], v34 offset:38912\n\t"
      "v_cvt_pk_bf16_f32 v105, v106, v107\n\t"
      "v_mfma_f32_16x16x32_bf16 v[128:131], a[0:3], a[248:251], v[128:131]\n\t"
      "ds_write_b64 v239, v[76:77] offset:6152\n\t"
      "v_mfma_f32_16x16x32_bf16 v[132:135], a[8:11], a[248:251], v[132:135]\n\t"
      "v_mad_u32_u24 v5, v5, s76, v238\n\t"
      "buffer_load_dwordx4 a[36:39], v12, s[52:55], s69 offen offset:1024 sc1\n\t"
      "v_mfma_f32_16x16x32_bf16 v[136:139], a[16:19], a[248:251], v[136:139]\n\t"
      "s_waitcnt vmcnt(9) lgkmcnt(5)\n\t"
      "ds_read_b128 a[248:251], v35 offset:38912\n\t"
      "v_mfma_f32_16x16x32_bf16 v[64:67], a[4:7], a[252:255], v[64:67]\n\t"
      "ds_write_b64 v243, v[80:81] offset:6144\n\t"
      "v_mfma_f32_16x16x32_bf16 v[72:75], a[12:15], a[252:255], v[72:75]\n\t"
      "buffer_load_dwordx4 a[44:47], v12, s[52:55], s69 offen offset:2048 sc1\n\t"
      "s_add_u32 s69, s69, 0xc00\n\t"
      "v_add_u32_e32 v5, 0xa80, v5\n\t"
      "v_mfma_f32_16x16x32_bf16 v[112:115], a[20:23], a[252:255], v[112:115]\n\t"
      "ds_read_b128 a[252:255], v33 offset:39936\n\t"
      "v_mfma_f32_16x16x32_bf16 v[116:119], a[4:7], a[88:91], v[116:119]\n\t"
      "ds_write_b64 v240, v[84:85] offset:6144\n\t"
      "v_mfma_f32_16x16x32_bf16 v[120:123], a[12:15], a[88:91], v[120:123]\n\t"
      "v_mfma_f32_16x16x32_bf16 v[124:127], a[20:23], a[88:91], v[124:127]\n\t"
      "buffer_load_dwordx4 a[48:51], v12, s[52:55], s69 offen sc1\n\t"
      "v_mfma_f32_16x16x32_bf16 v[128:131], a[4:7], a[92:95], v[128:131]\n\t"
      "ds_read_b128 a[88:91], v34 offset:39936\n\t"
      "v_mfma_f32_16x16x32_bf16 v[132:135], a[12:15], a[92:95], v[132:135]\n\t"
      "ds_write_b64 v240, v[88:89] offset:6152\n\t"
      "v_mfma_f32_16x16x32_bf16 v[136:139], a[20:23], a[92:95], v[136:139]\n\t"
      "buffer_load_dwordx4 a[56:59], v12, s[52:55], s69 offen offset:1024 sc1\n\t"
      "v_smfmac_f32_16x16x64_bf16 v[188:191], v[0:3], a[0:7], v236\n\t"
      "ds_read_b128 a[92:95], v35 offset:39936\n\t"
      "v_smfmac_f32_16x16x64_bf16 v[192:195], v[0:3], a[8:15], v236\n\t"
      "ds_write_b64 v244, v[92:93] offset:6144\n\t"
      "v_smfmac_f32_16x16x64_bf16 v[196:199], v[0:3], a[16:23], v236\n\t"
      "buffer_load_dwordx4 a[64:67], v12, s[52:55], s69 offen offset:2048 sc1\n\t"
      "s_add_u32 s69, s69, 0xc00\n\t"
      "ds_read2_b32 v[0:1], v4 offset0:64 offset1:66\n\t"
      "ds_read2_b32 v[2:3], v4 offset0:68 offset1:70\n\t"
      "s_waitcnt vmcnt(6) lgkmcnt(4)\n\t"
      "v_mfma_f32_16x16x32_bf16 v[64:67], a[24:27], a[240:243], v[64:67]\n\t"
      "buffer_load_dwordx4 a[52:55], v12, s[52:55], s69 offen sc1\n\t"
      "v_mfma_f32_16x16x32_bf16 v[72:75], a[32:35], a[240:243], v[72:75]\n\t"
      "buffer_load_dwordx4 a[60:63], v12, s[52:55], s69 offen offset:1024 sc1\n\t"
      "v_mfma_f32_16x16x32_bf16 v[112:115], a[40:43], a[240:243], v[112:115]\n\t"
      "buffer_load_dwordx4 a[68:71], v12, s[52:55], s69 offen offset:2048 sc1\n\t"
      "s_add_u32 s69, s69, 0xc00\n\t"
      "v_mfma_f32_16x16x32_bf16 v[116:119], a[24:27], a[244:247], v[116:119]\n\t"
      "ds_read_b128 a[240:243], v33 offset:40960\n\t"
      "v_mfma_f32_16x16x32_bf16 v[120:123], a[32:35], a[244:247], v[120:123]\n\t"
      "ds_write_b64 v241, v[96:97] offset:6144\n\t"
      "v_mfma_f32_16x16x32_bf16 v[124:127], a[40:43], a[244:247], v[124:127]\n\t"
      "buffer_load_dwordx4 a[192:195], v12, s[52:55], s69 offen sc1\n\t"
      "v_mfma_f32_16x16x32_bf16 v[128:131], a[24:27], a[248:251], v[128:131]\n\t"
      "ds_read_b128 a[244:247], v34 offset:40960\n\t"
      "v_mfma_f32_16x16x32_bf16 v[132:135], a[32:35], a[248:251], v[132:135]\n\t"
      "ds_write_b64 v241, v[100:101] offset:6152\n\t"
      "v_mfma_f32_16x16x32_bf16 v[136:139], a[40:43], a[248:251], v[136:139]\n\t"
      "s_waitcnt vmcnt(7)\n\t"
      "v_mfma_f32_16x16x32_bf16 v[64:67], a[28:31], a[252:255], v[64:67]\n\t"
      "buffer_load_dwordx4 a[200:203], v12, s[52:55], s69 offen offset:1024 sc1\n\t"
      "v_mfma_f32_16x16x32_bf16 v[72:75], a[36:39], a[252:255], v[72:75]\n\t"
      "ds_read_b128 a[248:251], v35 offset:40960\n\t"
      "v_mfma_f32_16x16x32_bf16 v[112:115], a[44:47], a[252:255], v[112:115]\n\t"
      "buffer_load_dwordx4 a[208:211], v12, s[52:55], s69 offen offset:2048 sc1\n\t"
      "s_add_u32 s69, s69, 0xc00\n\t"
      "v_mfma_f32_16x16x32_bf16 v[116:119], a[28:31], a[88:91], v[116:119]\n\t"
      "ds_read_b128 a[252:255], v33 offset:41984\n\t"
      "v_mfma_f32_16x16x32_bf16 v[120:123], a[36:39], a[88:91], v[120:123]\n\t"
      "ds_write_b64 v245, v[104:105] offset:6144\n\t"
      "v_mfma_f32_16x16x32_bf16 v[124:127], a[44:47], a[88:91], v[124:127]\n\t"
      "s_waitcnt lgkmcnt(4)\n\t"
      "buffer_load_dwordx4 a[196:199], v12, s[52:55], s69 offen sc1\n\t"
      "v_mfma_f32_16x16x32_bf16 v[128:131], a[28:31], a[92:95], v[128:131]\n\t"
      "ds_read_b128 a[88:91], v34 offset:41984\n\t"
      "v_mfma_f32_16x16x32_bf16 v[132:135], a[36:39], a[92:95], v[132:135]\n\t"
      "v_mfma_f32_16x16x32_bf16 v[136:139], a[44:47], a[92:95], v[136:139]\n\t"
      "ds_read_b128 a[92:95], v35 offset:41984\n\t"
      "v_smfmac_f32_16x16x64_bf16 v[188:191], v[8:11], a[24:31], v236\n\t"
      "buffer_load_dwordx4 a[204:207], v12, s[52:55], s69 offen offset:1024 sc1\n\t"
      "v_smfmac_f32_16x16x64_bf16 v[192:195], v[8:11], a[32:39], v236\n\t"
      "v_smfmac_f32_16x16x64_bf16 v[196:199], v[8:11], a[40:47], v236\n\t"
      "s_waitcnt lgkmcnt(2)\n\t"
      "s_barrier\n\t"
      "s_waitcnt vmcnt(8)\n\t"
      "v_mfma_f32_16x16x32_bf16 v[64:67], a[48:51], a[240:243], v[64:67]\n\t"
      "ds_read2_b32 v[8:9], v5 offset0:0 offset1:2\n\t"
      "v_mfma_f32_16x16x32_bf16 v[72:75], a[56:59], a[240:243], v[72:75]\n\t"
      "buffer_load_dwordx4 a[212:215], v12, s[52:55], s69 offen offset:2048 sc1\n\t"
      "s_add_u32 s69, s69, 0xc00\n\t"
      "v_mfma_f32_16x16x32_bf16 v[112:115], a[64:67], a[240:243], v[112:115]\n\t"
      "ds_read_b128 a[240:243], v33 offset:43008\n\t"
      "v_mfma_f32_16x16x32_bf16 v[116:119], a[48:51], a[244:247], v[116:119]\n\t"
      "v_mfma_f32_16x16x32_bf16 v[120:123], a[56:59], a[244:247], v[120:123]\n\t"
      "ds_read2_b32 v[10:11], v5 offset0:4 offset1:6\n\t"
      "v_mfma_f32_16x16x32_bf16 v[124:127], a[64:67], a[244:247], v[124:127]\n\t"
      "ds_read_b128 a[244:247], v34 offset:43008\n\t"
      "v_mfma_f32_16x16x32_bf16 v[128:131], a[48:51], a[248:251], v[128:131]\n\t"
      "v_mfma_f32_16x16x32_bf16 v[132:135], a[56:59], a[248:251], v[132:135]\n\t"
      "buffer_load_dwordx4 a[216:219], v12, s[52:55], s69 offen sc1\n\t"
      "v_mfma_f32_16x16x32_bf16 v[136:139], a[64:67], a[248:251], v[136:139]\n\t"
      "s_waitcnt vmcnt(7)\n\t"
      "ds_read_b128 a[248:251], v35 offset:43008\n\t"
      "v_mfma_f32_16x16x32_bf16 v[64:67], a[52:55], a[252:255], v[64:67]\n\t"
      "ds_read_b128 v[68:71], v247 offset:6144\n\t"
      "ds_read_b128 v[84:87], v248 offset:6144\n\t"
      "v_mfma_f32_16x16x32_bf16 v[72:75], a[60:63], a[252:255], v[72:75]\n\t"
      "buffer_load_dwordx4 a[224:227], v12, s[52:55], s69 offen offset:1024 sc1\n\t"
      "ds_read_b128 v[96:99], v249 offset:6144\n\t"
      "ds_read_b128 v[76:79], v250 offset:6144\n\t"
      "ds_read_b128 v[88:91], v251 offset:6144\n\t"
      "s_nop 0\n\t"
      "v_cvt_pk_bf16_f32 v64, v64, v65\n\t"
      "s_nop 1\n\t"
      "v_cvt_pk_bf16_f32 v72, v72, v73\n\t"
      "v_cvt_pk_bf16_f32 v65, v66, v67\n\t"
      "v_cvt_pk_bf16_f32 v73, v74, v75\n\t"
      "v_mfma_f32_16x16x32_bf16 v[112:115], a[68:71], a[252:255], v[112:115]\n\t"
      "s_waitcnt lgkmcnt(7)\n\t"
      "buffer_load_dwordx4 a[232:235], v12, s[52:55], s69 offen offset:2048 sc1\n\t"
      "v_mfma_f32_16x16x32_bf16 v[116:119], a[52:55], a[88:91], v[116:119]\n\t"
      "s_add_u32 s69, s69, 0xc00\n\t"
      "ds_read_b128 a[252:255], v33 offset:44032\n\t"
      "v_mfma_f32_16x16x32_bf16 v[120:123], a[60:63], a[88:91], v[120:123]\n\t"
      "ds_write_b64 v239, v[64:65] offset:12288\n\t"
      "v_mfma_f32_16x16x32_bf16 v[124:127], a[68:71], a[88:91], v[124:127]\n\t"
      "v_cvt_pk_bf16_f32 v112, v112, v113\n\t"
      "buffer_load_dwordx4 a[220:223], v12, s[52:55], s69 offen sc1\n\t"
      "v_mfma_f32_16x16x32_bf16 v[128:131], a[52:55], a[92:95], v[128:131]\n\t"
      "ds_read_b128 a[88:91], v34 offset:44032\n\t"
      "v_cvt_pk_bf16_f32 v116, v116, v117\n\t"
      "v_mfma_f32_16x16x32_bf16 v[132:135], a[60:63], a[92:95], v[132:135]\n\t"
      "v_cvt_pk_bf16_f32 v120, v120, v121\n\t"
      "v_cvt_pk_bf16_f32 v113, v114, v115\n\t"
      "v_mfma_f32_16x16x32_bf16 v[136:139], a[68:71], a[92:95], v[136:139]\n\t"
      "ds_read_b128 a[92:95], v35 offset:44032\n\t"
      "buffer_load_dwordx4 a[228:231], v12, s[52:55], s69 offen offset:1024 sc1\n\t"
      "v_smfmac_f32_16x16x64_bf16 v[188:191], v[0:3], a[48:55], v236\n\t"
      "v_cvt_pk_bf16_f32 v124, v124, v125\n\t"
      "v_cvt_pk_bf16_f32 v128, v128, v129\n\t"
      "v_smfmac_f32_16x16x64_bf16 v[192:195], v[0:3], a[56:63], v236\n\t"
      "v_cvt_pk_bf16_f32 v132, v132, v133\n\t"
      "v_cvt_pk_bf16_f32 v117, v118, v119\n\t"
      "v_smfmac_f32_16x16x64_bf16 v[196:199], v[0:3], a[64:71], v236\n\t"
      "s_waitcnt vmcnt(8)\n\t"
      "ds_read2_b32 v[0:1], v5 offset0:32 offset1:34\n\t"
      "v_mfma_f32_16x16x32_bf16 v[148:151], a[192:195], a[240:243], v[148:151]\n\t"
      "ds_read2_b32 v[2:3], v5 offset0:36 offset1:38\n\t"
      "buffer_load_dwordx4 a[236:239], v12, s[52:55], s69 offen offset:2048 sc1\n\t"
      "v_mfma_f32_16x16x32_bf16 v[152:155], a[200:203], a[240:243], v[152:155]\n\t"
      "s_add_u32 s69, s69, 0xc00\n\t"
      "v_cvt_pk_bf16_f32 v136, v136, v137\n\t"
      "v_cvt_pk_bf16_f32 v121, v122, v123\n\t"
      "v_mfma_f32_16x16x32_bf16 v[156:159], a[208:211], a[240:243], v[156:159]\n\t"
      "s_waitcnt lgkmcnt(4)\n\t"
      "ds_read_b128 a[240:243], v33 offset:45056\n\t"
      "v_mfma_f32_16x16x32_bf16 v[160:163], a[192:195], a[244:247], v[160:163]\n\t"
      "v_cvt_pk_bf16_f32 v125, v126, v127\n\t"
      "v_cvt_pk_bf16_f32 v129, v130, v131\n\t"
      "v_mfma_f32_16x16x32_bf16 v[164:167], a[200:203], a[244:247], v[164:167]\n\t"
      "buffer_load_dwordx4 a[0:3], v12, s[52:55], s69 offen sc1\n\t"
      "v_cvt_pk_bf16_f32 v133, v134, v135\n\t"
      "v_mfma_f32_16x16x32_bf16 v[168:171], a[208:211], a[244:247], v[168:171]\n\t"
      "ds_read_b128 a[244:247], v34 offset:45056\n\t"
      "v_cvt_pk_bf16_f32 v137, v138, v139\n\t"
      "v_mfma_f32_16x16x32_bf16 v[172:175], a[192:195], a[248:251], v[172:175]\n\t"
      "ds_write_b64 v239, v[72:73] offset:12296\n\t"
      "v_mfma_f32_16x16x32_bf16 v[176:179], a[200:203], a[248:251], v[176:179]\n\t"
      "v_mfma_f32_16x16x32_bf16 v[180:183], a[208:211], a[248:251], v[180:183]\n\t"
      "s_waitcnt vmcnt(7)\n\t"
      "buffer_load_dwordx4 a[8:11], v12, s[52:55], s69 offen offset:1024 sc1\n\t"
      "v_mfma_f32_16x16x32_bf16 v[148:151], a[196:199], a[252:255], v[148:151]\n\t"
      "ds_read_b128 a[248:251], v35 offset:45056\n\t"
      "v_mfma_f32_16x16x32_bf16 v[152:155], a[204:207], a[252:255], v[152:155]\n\t"
      "ds_write_b64 v243, v[112:113] offset:12288\n\t"
      "v_mfma_f32_16x16x32_bf16 v[156:159], a[212:215], a[252:255], v[156:159]\n\t"
      "s_waitcnt lgkmcnt(5)\n\t"
      "buffer_load_dwordx4 a[16:19], v12, s[52:55], s69 offen offset:2048 sc1\n\t"
      "v_mfma_f32_16x16x32_bf16 v[160:163], a[196:199], a[88:91], v[160:163]\n\t"
      "s_add_u32 s69, s69, 0xc00\n\t"
      "ds_read_b128 a[252:255], v33 offset:46080\n\t"
      "v_mfma_f32_16x16x32_bf16 v[164:167], a[204:207], a[88:91], v[164:167]\n\t"
      "ds_write_b64 v240, v[116:117] offset:12288\n\t"
      "v_mfma_f32_16x16x32_bf16 v[168:171], a[212:215], a[88:91], v[168:171]\n\t"
      "v_mfma_f32_16x16x32_bf16 v[172:175], a[196:199], a[92:95], v[172:175]\n\t"
      "ds_read_b128 a[88:91], v34 offset:46080\n\t"
      "buffer_load_dwordx4 a[4:7], v12, s[52:55], s69 offen sc1\n\t"
      "v_mfma_f32_16x16x32_bf16 v[176:179], a[204:207], a[92:95], v[176:179]\n\t"
      "ds_write_b64 v240, v[120:121] offset:12296\n\t"
      "v_mfma_f32_16x16x32_bf16 v[180:183], a[212:215], a[92:95], v[180:183]\n\t"
      "v_smfmac_f32_16x16x64_bf16 v[188:191], v[8:11], a[192:199], v236\n\t"
      "ds_read_b128 a[92:95], v35 offset:46080\n\t"
      "v_smfmac_f32_16x16x64_bf16 v[192:195], v[8:11], a[200:207], v236\n\t"
      "buffer_store_dwordx4 v[68:71], v252, s[48:51], s72 offen nt\n\t"
      "v_smfmac_f32_16x16x64_bf16 v[196:199], v[8:11], a[208:215], v236\n\t"
      "s_waitcnt vmcnt(8) lgkmcnt(5)\n\t"
      "ds_read2_b32 v[8:9], v5 offset0:64 offset1:66\n\t"
      "v_mfma_f32_16x16x32_bf16 v[148:151], a[216:219], a[240:243], v[148:151]\n\t"
      "ds_read2_b32 v[10:11], v5 offset0:68 offset1:70\n\t"
      "v_mfma_f32_16x16x32_bf16 v[152:155], a[224:227], a[240:243], v[152:155]\n\t"
      "buffer_load_dwordx4 a[12:15], v12, s[52:55], s69 offen offset:1024 sc1\n\t"
      "v_mfma_f32_16x16x32_bf16 v[156:159], a[232:235], a[240:243], v[156:159]\n\t"
      "ds_read_b128 a[240:243], v33 offset:47104\n\t"
      "v_mfma_f32_16x16x32_bf16 v[160:163], a[216:219], a[244:247], v[160:163]\n\t"
      "ds_write_b64 v244, v[124:125] offset:12288\n\t"
      "v_mfma_f32_16x16x32_bf16 v[164:167], a[224:227], a[244:247], v[164:167]\n\t"
      "buffer_load_dwordx4 a[20:23], v12, s[52:55], s69 offen offset:2048 sc1\n\t"
      "s_add_u32 s69, s69, 0xc00\n\t"
      "v_mfma_f32_16x16x32_bf16 v[168:171], a[232:235], a[244:247], v[168:171]\n\t"
      "ds_read_b128 a[244:247], v34 offset:47104\n\t"
      "v_mfma_f32_16x16x32_bf16 v[172:175], a[216:219], a[248:251], v[172:175]\n\t"
      "ds_write_b64 v241, v[128:129] offset:12288\n\t"
      "v_mfma_f32_16x16x32_bf16 v[176:179], a[224:227], a[248:251], v[176:179]\n\t"
      "v_mfma_f32_16x16x32_bf16 v[180:183], a[232:235], a[248:251], v[180:183]\n\t"
      "s_waitcnt vmcnt(7) lgkmcnt(6)\n\t"
      "ds_read_b128 a[248:251], v35 offset:47104\n\t"
      "v_mfma_f32_16x16x32_bf16 v[148:151], a[220:223], a[252:255], v[148:151]\n\t"
      "buffer_store_dwordx4 v[84:87], v253, s[48:51], s72 offen nt\n\t"
      "v_mfma_f32_16x16x32_bf16 v[152:155], a[228:231], a[252:255], v[152:155]\n\t"
      "buffer_store_dwordx4 v[96:99], v254, s[48:51], s72 offen nt\n\t"
      "v_mfma_f32_16x16x32_bf16 v[156:159], a[236:239], a[252:255], v[156:159]\n\t"
      "ds_read_b128 a[252:255], v33 offset:48128\n\t"
      "v_mfma_f32_16x16x32_bf16 v[160:163], a[220:223], a[88:91], v[160:163]\n\t"
      "ds_write_b64 v241, v[132:133] offset:12296\n\t"
      "v_mfma_f32_16x16x32_bf16 v[164:167], a[228:231], a[88:91], v[164:167]\n\t"
      "v_mfma_f32_16x16x32_bf16 v[168:171], a[236:239], a[88:91], v[168:171]\n\t"
      "ds_write_b64 v245, v[136:137] offset:12288\n\t"
      "v_mfma_f32_16x16x32_bf16 v[172:175], a[220:223], a[92:95], v[172:175]\n\t"
      "buffer_store_dwordx4 v[76:79], v36, s[48:51], s72 offen nt\n\t"
      "v_mfma_f32_16x16x32_bf16 v[176:179], a[228:231], a[92:95], v[176:179]\n\t"
      "ds_read_b128 a[88:91], v34 offset:48128\n\t"
      "v_mfma_f32_16x16x32_bf16 v[180:183], a[236:239], a[92:95], v[180:183]\n\t"
      "buffer_store_dwordx4 v[88:91], v27, s[48:51], s72 offen nt\n\t"
      "v_smfmac_f32_16x16x64_bf16 v[188:191], v[0:3], a[216:223], v236\n\t"
      "ds_read_b128 a[92:95], v35 offset:48128\n\t"
      "v_smfmac_f32_16x16x64_bf16 v[192:195], v[0:3], a[224:231], v236\n\t"
      "v_smfmac_f32_16x16x64_bf16 v[196:199], v[0:3], a[232:239], v236\n\t"
      "s_waitcnt lgkmcnt(2)\n\t"
      "s_barrier\n\t"
      "s_waitcnt vmcnt(8)\n\t"
      "v_mfma_f32_16x16x32_bf16 v[148:151], a[0:3], a[240:243], v[148:151]\n\t"
      "ds_read_b128 v[64:67], v247 offset:12288\n\t"
      "v_mfma_f32_16x16x32_bf16 v[152:155], a[8:11], a[240:243], v[152:155]\n\t"
      "v_mfma_f32_16x16x32_bf16 v[156:159], a[16:19], a[240:243], v[156:159]\n\t"
      "ds_read_b128 v[116:119], v248 offset:12288\n\t"
      "v_mfma_f32_16x16x32_bf16 v[160:163], a[0:3], a[244:247], v[160:163]\n\t"
      "v_mfma_f32_16x16x32_bf16 v[164:167], a[8:11], a[244:247], v[164:167]\n\t"
      "ds_read_b128 v[128:131], v249 offset:12288\n\t"
      "v_mfma_f32_16x16x32_bf16 v[168:171], a[16:19], a[244:247], v[168:171]\n\t"
      "v_mfma_f32_16x16x32_bf16 v[172:175], a[0:3], a[248:251], v[172:175]\n\t"
      "ds_read_b128 v[72:75], v250 offset:12288\n\t"
      "v_mfma_f32_16x16x32_bf16 v[176:179], a[8:11], a[248:251], v[176:179]\n\t"
      "v_mfma_f32_16x16x32_bf16 v[180:183], a[16:19], a[248:251], v[180:183]\n\t"
      "s_waitcnt vmcnt(7)\n\t"
      "ds_read_b128 v[120:123], v251 offset:12288\n\t"
      "v_smfmac_f32_16x16x64_bf16 v[188:191], v[8:11], a[0:7], v236\n\t"
      "s_waitcnt lgkmcnt(4)\n\t"
      "buffer_store_dwordx4 v[64:67], v252, s[48:51], s73 offen nt\n\t"
      "s_waitcnt vmcnt(5)\n\t"
      "v_smfmac_f32_16x16x64_bf16 v[192:195], v[8:11], a[8:15], v236\n\t"
      "s_waitcnt lgkmcnt(3)\n\t"
      "buffer_store_dwordx4 v[116:119], v253, s[48:51], s73 offen nt\n\t"
      "s_nop 5\n\t"
      "v_add_f32_e32 v188, v188, v189\n\t"
      "v_add_f32_e32 v190, v190, v191\n\t"
      "v_cvt_pk_bf16_f32 v188, v188, v190\n\t"
      "s_nop 0\n\t"
      "v_add_f32_e32 v192, v192, v193\n\t"
      "v_add_f32_e32 v194, v194, v195\n\t"
      "v_cvt_pk_bf16_f32 v192, v192, v194\n\t"
      "ds_write_b16 v242, v188\n\t"
      "v_smfmac_f32_16x16x64_bf16 v[196:199], v[8:11], a[16:23], v236\n\t"
      "s_waitcnt lgkmcnt(2)\n\t"
      "buffer_store_dwordx4 v[128:131], v254, s[48:51], s73 offen nt\n\t"
      "v_mfma_f32_16x16x32_bf16 v[148:151], a[4:7], a[252:255], v[148:151]\n\t"
      "ds_write_b16_d16_hi v242, v188 offset:384\n\t"
      "v_mfma_f32_16x16x32_bf16 v[152:155], a[12:15], a[252:255], v[152:155]\n\t"
      "ds_write_b16 v242, v192 offset:8\n\t"
      "v_mfma_f32_16x16x32_bf16 v[156:159], a[20:23], a[252:255], v[156:159]\n\t"
      "ds_write_b16_d16_hi v242, v192 offset:392\n\t"
      "v_mfma_f32_16x16x32_bf16 v[160:163], a[4:7], a[88:91], v[160:163]\n\t"
      "buffer_store_dwordx4 v[72:75], v36, s[48:51], s73 offen nt\n\t"
      "s_nop 1\n\t"
      "v_add_f32_e32 v196, v196, v197\n\t"
      "v_mfma_f32_16x16x32_bf16 v[164:167], a[12:15], a[88:91], v[164:167]\n\t"
      "v_add_f32_e32 v198, v198, v199\n\t"
      "v_cvt_pk_bf16_f32 v148, v148, v149\n\t"
      "v_mfma_f32_16x16x32_bf16 v[168:171], a[20:23], a[88:91], v[168:171]\n\t"
      "v_cvt_pk_bf16_f32 v152, v152, v153\n\t"
      "v_cvt_pk_bf16_f32 v156, v156, v157\n\t"
      "v_mfma_f32_16x16x32_bf16 v[172:175], a[4:7], a[92:95], v[172:175]\n\t"
      "v_cvt_pk_bf16_f32 v160, v160, v161\n\t"
      "s_nop 0\n\t"
      "v_cvt_pk_bf16_f32 v164, v164, v165\n\t"
      "v_mfma_f32_16x16x32_bf16 v[176:179], a[12:15], a[92:95], v[176:179]\n\t"
      "v_cvt_pk_bf16_f32 v196, v196, v198\n\t"
      "v_cvt_pk_bf16_f32 v168, v168, v169\n\t"
      "v_mfma_f32_16x16x32_bf16 v[180:183], a[20:23], a[92:95], v[180:183]\n\t"
      "v_cvt_pk_bf16_f32 v149, v150, v151\n\t"
      "v_cvt_pk_bf16_f32 v172, v172, v173\n\t"
      "v_cvt_pk_bf16_f32 v153, v154, v155\n\t"
      "v_cvt_pk_bf16_f32 v157, v158, v159\n\t"
      "s_nop 0\n\t"
      "v_cvt_pk_bf16_f32 v176, v176, v177\n\t"
      "v_cvt_pk_bf16_f32 v161, v162, v163\n\t"
      "v_cvt_pk_bf16_f32 v165, v166, v167\n\t"
      "v_cvt_pk_bf16_f32 v180, v180, v181\n\t"
      "v_cvt_pk_bf16_f32 v169, v170, v171\n\t"
      "v_cvt_pk_bf16_f32 v173, v174, v175\n\t"
      "v_cvt_pk_bf16_f32 v177, v178, v179\n\t"
      "v_cvt_pk_bf16_f32 v181, v182, v183\n\t"
      "ds_write_b16 v246, v196 offset:64\n\t"
      "ds_write_b16_d16_hi v246, v196 offset:448\n\t"
      "ds_write_b64 v239, v[148:149] offset:24576\n\t"
      "ds_write_b64 v239, v[152:153] offset:24584\n\t"
      "ds_write_b64 v243, v[156:157] offset:24576\n\t"
      "ds_write_b64 v240, v[160:161] offset:24576\n\t"
      "ds_write_b64 v240, v[164:165] offset:24584\n\t"
      "ds_write_b64 v244, v[168:169] offset:24576\n\t"
      "ds_write_b64 v241, v[172:173] offset:24576\n\t"
      "ds_write_b64 v241, v[176:177] offset:24584\n\t"
      "ds_write_b64 v245, v[180:181] offset:24576\n\t"
      "s_waitcnt lgkmcnt(11)\n\t"
      "buffer_store_dwordx4 v[120:123], v27, s[48:51], s73 offen nt\n\t"
      "s_waitcnt lgkmcnt(0)\n\t"
      "s_barrier\n\t"
      "ds_read_b128 v[148:151], v247 offset:24576\n\t"
      "ds_read_b128 v[160:163], v248 offset:24576\n\t"
      "ds_read_b128 v[172:175], v249 offset:24576\n\t"
      "ds_read_b128 v[152:155], v250 offset:24576\n\t"
      "ds_read_b128 v[164:167], v251 offset:24576\n\t"
      "ds_read_b128 v[188:191], v6\n\t"
      "s_waitcnt lgkmcnt(5)\n\t"
      "buffer_store_dwordx4 v[148:151], v252, s[48:51], s74 offen nt\n\t"
      "s_waitcnt lgkmcnt(0)\n\t"
      "buffer_store_dwordx4 v[188:191], v7, s[48:51], s71 offen nt\n\t"
      "buffer_store_dwordx4 v[160:163], v253, s[48:51], s74 offen nt\n\t"
      "buffer_store_dwordx4 v[172:175], v254, s[48:51], s74 offen nt\n\t"
      "buffer_store_dwordx4 v[152:155], v36, s[48:51], s74 offen nt\n\t"
      "buffer_store_dwordx4 v[164:167], v27, s[48:51], s74 offen nt\n\t"
      "Lend_%=:\n\t"
      :
      : "v"(threadIdx.x), "s"(__builtin_amdgcn_kernarg_segment_ptr()), "s"(blockIdx.x)
      : "v0", "v1", "v2", "v3", "v4", "v5", "v6", "v7", "v8", "v9", "v10", "v11", "v12", "v13", "v14", "v15", "v16", "v17", "v18", "v19", "v20", "v21", "v22", "v23", "v24", "v25", "v26", "v27", "v28", "v29", "v30", "v31", "v32", "v33", "v34", "v35", "v36", "v37", "v38", "v39", "v40", "v41", "v42", "v43", "v44", "v45", "v46", "v47", "v48", "v49", "v50", "v51", "v52", "v53", "v54", "v55", "v56", "v57", "v58", "v59", "v60", "v61", "v62", "v63", "v64", "v65", "v66", "v67", "v68", "v69", "v70", "v71", "v72", "v73", "v74", "v75", "v76", "v77", "v78", "v79", "v80", "v81", "v82", "v83", "v84", "v85", "v86", "v87", "v88", "v89", "v90", "v91", "v92", "v93", "v94", "v95", "v96", "v97", "v98", "v99", "v100", "v101", "v102", "v103", "v104", "v105", "v106", "v107", "v108", "v109", "v110", "v111", "v112", "v113", "v114", "v115", "v116", "v117", "v118", "v119", "v120", "v121", "v122", "v123", "v124", "v125", "v126", "v127", "v128", "v129", "v130", "v131", "v132", "v133", "v134", "v135", "v136", "v137", "v138", "v139", "v140", "v141", "v142", "v143", "v144", "v145", "v146", "v147", "v148", "v149", "v150", "v151", "v152", "v153", "v154", "v155", "v156", "v157", "v158", "v159", "v160", "v161", "v162", "v163", "v164", "v165", "v166", "v167", "v168", "v169", "v170", "v171", "v172", "v173", "v174", "v175", "v176", "v177", "v178", "v179", "v180", "v181", "v182", "v183", "v184", "v185", "v186", "v187", "v188", "v189", "v190", "v191", "v192", "v193", "v194", "v195", "v196", "v197", "v198", "v199", "v200", "v201", "v202", "v203", "v204", "v205", "v206", "v207", "v208", "v209", "v210", "v211", "v212", "v213", "v214", "v215", "v216", "v217", "v218", "v219", "v220", "v221", "v222", "v223", "v224", "v225", "v226", "v227", "v228", "v229", "v230", "v231", "v232", "v233", "v234", "v235", "v236", "v237", "v238", "v239", "v240", "v241", "v242", "v243", "v244", "v245", "v246", "v247", "v248", "v249", "v250", "v251", "v252", "v253", "v254", "a0", "a1", "a2", "a3", "a4", "a5", "a6", "a7", "a8", "a9", "a10", "a11", "a12", "a13", "a14", "a15", "a16", "a17", "a18", "a19", "a20", "a21", "a22", "a23", "a24", "a25", "a26", "a27", "a28", "a29", "a30", "a31", "a32", "a33", "a34", "a35", "a36", "a37", "a38", "a39", "a40", "a41", "a42", "a43", "a44", "a45", "a46", "a47", "a48", "a49", "a50", "a51", "a52", "a53", "a54", "a55", "a56", "a57", "a58", "a59", "a60", "a61", "a62", "a63", "a64", "a65", "a66", "a67", "a68", "a69", "a70", "a71", "a72", "a73", "a74", "a75", "a76", "a77", "a78", "a79", "a80", "a81", "a82", "a83", "a84", "a85", "a86", "a87", "a88", "a89", "a90", "a91", "a92", "a93", "a94", "a95", "a96", "a97", "a98", "a99", "a100", "a101", "a102", "a103", "a104", "a105", "a106", "a107", "a108", "a109", "a110", "a111", "a112", "a113", "a114", "a115", "a116", "a117", "a118", "a119", "a120", "a121", "a122", "a123", "a124", "a125", "a126", "a127", "a128", "a129", "a130", "a131", "a132", "a133", "a134", "a135", "a136", "a137", "a138", "a139", "a140", "a141", "a142", "a143", "a144", "a145", "a146", "a147", "a148", "a149", "a150", "a151", "a152", "a153", "a154", "a155", "a156", "a157", "a158", "a159", "a160", "a161", "a162", "a163", "a164", "a165", "a166", "a167", "a168", "a169", "a170", "a171", "a172", "a173", "a174", "a175", "a176", "a177", "a178", "a179", "a180", "a181", "a182", "a183", "a184", "a185", "a186", "a187", "a188", "a189", "a190", "a191", "a192", "a193", "a194", "a195", "a196", "a197", "a198", "a199", "a200", "a201", "a202", "a203", "a204", "a205", "a206", "a207", "a208", "a209", "a210", "a211", "a212", "a213", "a214", "a215", "a216", "a217", "a218", "a219", "a220", "a221", "a222", "a223", "a224", "a225", "a226", "a227", "a228", "a229", "a230", "a231", "a232", "a233", "a234", "a235", "a236", "a237", "a238", "a239", "a240", "a241", "a242", "a243", "a244", "a245", "a246", "a247", "a248", "a249", "a250", "a251", "a252", "a253", "a254", "a255", "s4", "s5", "s6", "s7", "s8", "s9", "s10", "s11", "s12", "s13", "s14", "s15", "s34", "s35", "s36", "s37", "s38", "s39", "s40", "s41", "s42", "s43", "s44", "s45", "s46", "s47", "s48", "s49", "s50", "s51", "s52", "s53", "s54", "s55", "s56", "s57", "s58", "s59", "s60", "s61", "s62", "s63", "s64", "s65", "s66", "s67", "s68", "s69", "s70", "s71", "s72", "s73", "s74", "s75", "s76", "s77", "s78", "s79", "s80", "s81", "s82", "s83", "s84", "s85", "s86", "s87", "s88", "s89", "s90", "s91", "s92", "s93", "s94", "s95", "s96", "s97", "s98", "s99", "vcc", "scc", "memory");
}
}

extern "C" void kernel_launch(void* const* d_in, const int* in_sizes, int n_in, void* d_out, int out_size,
                              void* d_ws, size_t ws_size, hipStream_t stream) {
  (void)in_sizes; (void)n_in; (void)out_size; (void)ws_size;
  (void)hipFuncSetAttribute((const void*)fpn_main_kernel, hipFuncAttributeMaxDynamicSharedMemorySize, 163840);
  fpn_main_kernel<<<256, 256, 163840, stream>>>(d_in[0], d_in[1], d_out, d_ws, d_in[2], d_in[3]);
}
```
